```python
import torch

def _softmax(x, axis=-1):
    return torch.softmax(x, dim=axis)
def _gelu(x):
    return torch.nn.functional.gelu(x, approximate="tanh")
def _silu(x):
    return torch.nn.functional.silu(x)
def _sigmoid(x):
    return torch.sigmoid(x)
def _layernorm(x, axis=-1, eps=1e-5):
    m = x.mean(dim=axis, keepdim=True)
    v = x.var(dim=axis, keepdim=True, unbiased=False)
    return (x - m) / torch.sqrt(v + eps)
def _scatter_add(tgt, idx, src, axis=0):
    return tgt.clone().index_add_(axis, idx, src)
def _segment_sum(src, seg, n):
    out = torch.zeros((n,) + src.shape[1:], dtype=src.dtype)
    return out.index_add_(0, seg, src)
def _pad(x, pw):
    flat = tuple(v for pair in reversed(pw) for v in pair)
    return torch.nn.functional.pad(x, flat)

kernel_name = 'gated_mix__linear_kernel_hedgehog__ssm_conv_s4__prompt'

def setup_inputs():

    _w = (lambda a: (lambda t: t.to(torch.bfloat16) if t.is_floating_point() else t)(torch.as_tensor(a)))
    def _A_setup():
        B, S, D, H = 1, 2048, 448, 16
        Dh = D // H
        return {
            "x": _w((lambda *s: torch.randn(*s, dtype=torch.bfloat16) * 0.05)(B, S, D)),
            "wqkv": _w((lambda *s: torch.randn(*s, dtype=torch.bfloat16) * 0.8)(D, 3 * D)),
            "w_phi": _w((lambda *s: torch.randn(*s, dtype=torch.bfloat16) * 0.8)(H, Dh, Dh) * (Dh ** -0.5)),
            "wo": _w((lambda *s: torch.randn(*s, dtype=torch.bfloat16) * 0.05)(D, D)),
        }
    _A_in = _A_setup()

    def _B_setup():
        B, L, D, N = 1, 2048, 448, 256
        A_re = -(lambda _x: torch.exp(torch.as_tensor(_x)))((lambda *s: torch.randn(*s, dtype=torch.bfloat16) * 6.4)(D, N))
        A_im = torch.pi * (lambda *s: torch.randn(*s, dtype=torch.bfloat16) * 0.05)(D, N)
        B_re = (lambda *s: torch.randn(*s, dtype=torch.bfloat16) * 0.1)(D, N)
        B_im = (lambda *s: torch.randn(*s, dtype=torch.bfloat16) * 0.1)(D, N)
        C_re = (lambda *s: torch.randn(*s, dtype=torch.bfloat16) * 0.1)(D, N)
        C_im = (lambda *s: torch.randn(*s, dtype=torch.bfloat16) * 0.1)(D, N)
        log_dt = (lambda *s: torch.randn(*s, dtype=torch.bfloat16) * 0.05)(D) * 0.5 - 4.0
        Dskip = (lambda *s: torch.randn(*s, dtype=torch.bfloat16) * 0.05)(D)
        return {
            "u": _w((lambda *s: torch.randn(*s, dtype=torch.bfloat16) * 0.05)(B, L, D)),
            "A_re": _w(A_re),
            "A_im": _w(A_im),
            "B_re": _w(B_re),
            "B_im": _w(B_im),
            "C_re": _w(C_re),
            "C_im": _w(C_im),
            "log_dt": _w(log_dt),
            "Dskip": _w(Dskip),
        }
    _B_in = _B_setup()

    return {
        "A_x": _A_in["x"],
        "A_wqkv": _A_in["wqkv"],
        "A_w_phi": _A_in["w_phi"],
        "A_wo": _A_in["wo"],
        "B_u": _B_in["u"],
        "B_A_re": _B_in["A_re"],
        "B_A_im": _B_in["A_im"],
        "B_B_re": _B_in["B_re"],
        "B_B_im": _B_in["B_im"],
        "B_C_re": _B_in["C_re"],
        "B_C_im": _B_in["C_im"],
        "B_log_dt": _B_in["log_dt"],
        "B_Dskip": _B_in["Dskip"],
        "gate_w": _w((lambda *s: torch.randn(*s, dtype=torch.bfloat16) * 0.05)(448) * 16.0),
    }

def reference(A_x, A_wqkv, A_w_phi, A_wo, B_u, B_A_re, B_A_im, B_B_re, B_B_im, B_C_re, B_C_im, B_log_dt, B_Dskip, gate_w):
    def _A_ref(x, wqkv, w_phi, wo):
        B, S, D = x.shape
        H = 16; Dh = D // H
        qkv = (lambda x, *d: x.permute(*d))((x @ wqkv).reshape(B, S, 3, H, Dh), 2, 0, 3, 1, 4)
        q, k, v = qkv[0], qkv[1], qkv[2]
        pq = torch.einsum("bhsd,hdf->bhsf", q, w_phi)
        pk = torch.einsum("bhsd,hdf->bhsf", k, w_phi)
        phi_q = torch.concatenate([_softmax(pq, axis=-1), _softmax(-pq, axis=-1)], axis=-1)
        phi_k = torch.concatenate([_softmax(pk, axis=-1), _softmax(-pk, axis=-1)], axis=-1)
        kv = torch.einsum("bhsf,bhsd->bhfd", phi_k, v)
        z  = torch.einsum("bhsf->bhf", phi_k)
        num = torch.einsum("bhqf,bhfd->bhqd", phi_q, kv)
        den = torch.einsum("bhqf,bhf->bhq", phi_q, z)[..., None] + 1e-6
        o = num / den
        return (lambda x, *d: x.permute(*d))(o, 0, 2, 1, 3).reshape(B, S, D) @ wo

    def _B_ref(u, A_re, A_im, B_re, B_im, C_re, C_im, log_dt, Dskip):
        Bsz, L, D = u.shape
        dt = torch.exp(log_dt)[:, None]
        A = A_re + 1j * A_im
        Bc = B_re + 1j * B_im
        Cc = C_re + 1j * C_im
        logdA = dt * A
        dB = dt * Bc
        l = (lambda x, dt: x.to(dt))(torch.arange(L), A_re.dtype)
        P = torch.exp(logdA[:, :, None] * l[None, None, :])
        K = torch.einsum("dn,dnl,dn->dl", Cc, P, dB).real
        L2 = 2 * L
        Kp = _pad(K, ((0, 0), (0, L)))
        up = (lambda x, *d: x.permute(*d))(u, 0, 2, 1)
        up = _pad(up, ((0, 0), (0, 0), (0, L)))
        Kf = (lambda x, axes=(-2,-1): torch.fft.rfft2(x.float(), dim=axes))(Kp[:, None, :], axes=(-2, -1))
        Uf = (lambda x, axes=(-2,-1): torch.fft.rfft2(x.float(), dim=axes))(up[:, :, None, :], axes=(-2, -1))
        Yf = Uf * Kf[None, :, :, :]
        y = (lambda x, s, axes=(-2,-1): torch.fft.irfft2(x, s=s, dim=axes).to(torch.bfloat16))(Yf, (1, L2), axes=(-2, -1))[:, :, 0, :L]
        y = (lambda x, *d: x.permute(*d))(y, 0, 2, 1) + u * Dskip[None, None, :]
        return y

    x = A_x
    g = _sigmoid((x * gate_w).sum(-1, keepdims=True))
    a = _A_ref(x, A_wqkv, A_w_phi, A_wo)
    b = _B_ref(x, B_A_re, B_A_im, B_B_re, B_B_im, B_C_re, B_C_im, B_log_dt, B_Dskip)
    return g * a + (1 - g) * b
```

```cpp
#include <hip/hip_runtime.h>
#include <hip/hip_bf16.h>
#include <stdint.h>

typedef unsigned short u16;
__device__ __forceinline__ float bf2f(u16 v) { return __uint_as_float(((uint32_t)v) << 16); }
__device__ __forceinline__ u16 f2bf(float f) { uint32_t u = __float_as_uint(f); u += 0x7fffu + ((u >> 16) & 1u); return (u16)(u >> 16); }
__device__ __forceinline__ float rbf(float f) { return bf2f(f2bf(f)); }

constexpr int S = 2048, D = 448, H = 16, DH = 28, N = 256;

__global__ void k_qkv(const u16* x, const u16* w, u16* qkv) {
  int col = blockIdx.x * 64 + threadIdx.x; int s = blockIdx.y;
  if (col >= 3 * D) return;
  float acc = 0.f;
  for (int k = 0; k < D; ++k) acc += bf2f(x[s * D + k]) * bf2f(w[k * 3 * D + col]);
  qkv[s * 3 * D + col] = f2bf(acc);
}
__global__ void k_phi(const u16* qkv, const u16* wphi, u16* phi) {
  int s = blockIdx.x; int which = blockIdx.y; int h = threadIdx.x; if (h >= H) return;
  float p[DH];
  for (int f = 0; f < DH; ++f) {
    float acc = 0.f;
    for (int d = 0; d < DH; ++d) acc += bf2f(qkv[s * 3 * D + which * D + h * DH + d]) * bf2f(wphi[(h * DH + d) * DH + f]);
    p[f] = rbf(acc);
  }
  float mx = -1e30f, mn = 1e30f;
  for (int f = 0; f < DH; ++f) { mx = fmaxf(mx, p[f]); mn = fminf(mn, p[f]); }
  float s1 = 0.f, s2 = 0.f;
  for (int f = 0; f < DH; ++f) { s1 += expf(p[f] - mx); s2 += expf(mn - p[f]); }
  u16* o = phi + (((size_t)which * H + h) * S + s) * 56;
  for (int f = 0; f < DH; ++f) { o[f] = f2bf(expf(p[f] - mx) / s1); o[28 + f] = f2bf(expf(mn - p[f]) / s2); }
}
__global__ void k_kv(const u16* qkv, const u16* phi, float* kv) {
  int h = blockIdx.x; int f = blockIdx.y; int d = threadIdx.x; if (d > 28) return;
  const u16* pk = phi + (((size_t)1 * H + h) * S) * 56;
  float acc = 0.f;
  for (int s = 0; s < S; ++s) {
    float v = d < 28 ? bf2f(qkv[s * 3 * D + 2 * D + h * DH + d]) : 1.f;
    acc += bf2f(pk[s * 56 + f]) * v;
  }
  kv[(h * 56 + f) * 29 + d] = rbf(acc);
}
__global__ void k_o(const u16* phi, const float* kv, u16* o) {
  int s = blockIdx.x; int c = threadIdx.x; if (c >= D) return;
  int h = c / DH, d = c % DH;
  const u16* pq = phi + (((size_t)0 * H + h) * S + s) * 56;
  float num = 0.f, den = 0.f;
  for (int f = 0; f < 56; ++f) { float q = bf2f(pq[f]); num += q * kv[(h * 56 + f) * 29 + d]; den += q * kv[(h * 56 + f) * 29 + 28]; }
  num = rbf(num); den = rbf(rbf(den) + 1e-6f);
  o[s * D + c] = f2bf(num / den);
}
__global__ void k_kgen(const u16* Are, const u16* Aim, const u16* Bre, const u16* Bim, const u16* Cre, const u16* Cim, const u16* logdt, float* K) {
  int d = blockIdx.y; int l = blockIdx.x * 256 + threadIdx.x;
  float dt = rbf(expf(bf2f(logdt[d])));
  float lb = rbf((float)l);
  float acc = 0.f;
  for (int n = 0; n < N; ++n) {
    float ar = dt * bf2f(Are[d * N + n]), ai = dt * bf2f(Aim[d * N + n]);
    float br = dt * bf2f(Bre[d * N + n]), bi = dt * bf2f(Bim[d * N + n]);
    float cr = bf2f(Cre[d * N + n]), ci = bf2f(Cim[d * N + n]);
    float wr = cr * br - ci * bi, wi = cr * bi + ci * br;
    float e = expf(ar * lb); float sn, cs; sincosf(ai * lb, &sn, &cs);
    acc += e * (wr * cs - wi * sn);
  }
  K[d * S + l] = acc;
}
__global__ void k_final(const u16* x, const u16* o, const u16* wo, const float* K, const u16* dskip, const u16* gw, u16* out) {
  int s = blockIdx.x; int c = threadIdx.x;
  __shared__ float red[512];
  float gp = c < D ? rbf(bf2f(x[s * D + c]) * bf2f(gw[c])) : 0.f;
  red[c] = gp; __syncthreads();
  for (int st = 256; st > 0; st >>= 1) { if (c < st) red[c] += red[c + st]; __syncthreads(); }
  float g = rbf(red[0]); g = rbf(1.f / (1.f + expf(-g)));
  if (c >= D) return;
  float a = 0.f;
  for (int k = 0; k < D; ++k) a += bf2f(o[s * D + k]) * bf2f(wo[k * D + c]);
  a = rbf(a);
  float y = 0.f;
  for (int j = 0; j <= s; ++j) y += K[c * S + j] * bf2f(x[(s - j) * D + c]);
  y = rbf(y);
  float b = rbf(y + rbf(bf2f(x[s * D + c]) * bf2f(dskip[c])));
  float r = rbf(rbf(g * a) + rbf(rbf(1.f - g) * b));
  out[s * D + c] = f2bf(r);
}

extern "C" void kernel_launch(void* const* d_in, const int* in_sizes, int n_in, void* d_out, int out_size, void* d_ws, size_t ws_size, hipStream_t stream) {
  const u16* x = (const u16*)d_in[0]; const u16* wqkv = (const u16*)d_in[1]; const u16* wphi = (const u16*)d_in[2]; const u16* wo = (const u16*)d_in[3];
  const u16* Are = (const u16*)d_in[5]; const u16* Aim = (const u16*)d_in[6]; const u16* Bre = (const u16*)d_in[7]; const u16* Bim = (const u16*)d_in[8];
  const u16* Cre = (const u16*)d_in[9]; const u16* Cim = (const u16*)d_in[10]; const u16* logdt = (const u16*)d_in[11]; const u16* dskip = (const u16*)d_in[12]; const u16* gw = (const u16*)d_in[13];
  char* ws = (char*)d_ws;
  u16* qkv = (u16*)ws; ws += (size_t)S * 3 * D * 2;
  u16* phi = (u16*)ws; ws += (size_t)2 * H * S * 56 * 2;
  float* kv = (float*)ws; ws += (size_t)H * 56 * 29 * 4 + 256; ws = (char*)(((uintptr_t)ws + 255) & ~(uintptr_t)255);
  u16* o = (u16*)ws; ws += (size_t)S * D * 2;
  float* K = (float*)ws; ws += (size_t)D * S * 4;
  k_qkv<<<dim3(21, S), 64, 0, stream>>>(x, wqkv, qkv);
  k_phi<<<dim3(S, 2), 64, 0, stream>>>(qkv, wphi, phi);
  k_kv<<<dim3(H, 56), 64, 0, stream>>>(qkv, phi, kv);
  k_o<<<S, 448, 0, stream>>>(phi, kv, o);
  k_kgen<<<dim3(8, D), 256, 0, stream>>>(Are, Aim, Bre, Bim, Cre, Cim, logdt, K);
  k_final<<<S, 512, 0, stream>>>(x, o, wo, K, dskip, gw, (u16*)d_out);
}
```

```cpp
#include <hip/hip_runtime.h>
#include <stdint.h>

typedef unsigned short u16;
typedef unsigned int u32;
typedef short s16x4 __attribute__((ext_vector_type(4)));
typedef __bf16 bf16x8 __attribute__((ext_vector_type(8)));
typedef __bf16 bf16x2 __attribute__((ext_vector_type(2)));
typedef float f32x4 __attribute__((ext_vector_type(4)));
typedef float f32x2 __attribute__((ext_vector_type(2)));
typedef u32 u32x4 __attribute__((ext_vector_type(4)));
typedef u32 u32x2 __attribute__((ext_vector_type(2)));
#define LAS __attribute__((address_space(3)))
#define GAS __attribute__((address_space(1)))
#define DI __device__ __forceinline__

constexpr int S = 2048, D = 448, H = 16, DH = 28, NM = 256;

constexpr size_t WS_PHIQ = 0;
constexpr size_t WS_KVP = WS_PHIQ + (size_t)H * 128 * 2 * 1024;
constexpr size_t WS_O = WS_KVP + (size_t)H * 16 * 8192;
constexpr size_t WS_KR = WS_O + (size_t)S * D * 2;
constexpr size_t WS_XT = WS_KR + (size_t)D * 2304 * 2;
constexpr size_t WS_YT = WS_XT + (size_t)D * S * 2;
constexpr size_t WS_WOF = WS_YT + (size_t)D * S * 2;
constexpr size_t WS_G = WS_WOF + (size_t)28 * 14 * 1024;
constexpr size_t WS_END = WS_G + (size_t)S * 4;

DI float bf2f(u16 v) { return __uint_as_float(((u32)v) << 16); }
DI float bflo(u32 v) { return __uint_as_float(v << 16); }
DI float bfhi(u32 v) { return __uint_as_float(v & 0xffff0000u); }
DI u32 pk2(float lo, float hi) { f32x2 p = {lo, hi}; bf16x2 q = __builtin_convertvector(p, bf16x2); return __builtin_bit_cast(u32, q); }
DI float smul(float a, float b) { float r; asm("v_mul_f32 %0, %1, %2" : "=v"(r) : "v"(a), "v"(b)); return r; }
DI float rbf(float f) { return bflo(pk2(f, f) ); }
DI f32x4 mfma16(u32x4 a, u32x4 b, f32x4 c) {
  return __builtin_amdgcn_mfma_f32_16x16x32_bf16(__builtin_bit_cast(bf16x8, a), __builtin_bit_cast(bf16x8, b), c, 0, 0, 0);
}
DI u32x2 trread(const char* lds_base, u32 off) {
  s16x4 t = __builtin_amdgcn_ds_read_tr16_b64_v4i16((LAS s16x4*)((LAS char*)lds_base + off));
  return __builtin_bit_cast(u32x2, t);
}
DI void glds16(const void* g, char* lds_dst) {
  __builtin_amdgcn_global_load_lds((const GAS void*)g, (LAS void*)lds_dst, 16, 0, 0);
}
#define WAIT_V(n) asm volatile("s_waitcnt vmcnt(" #n ")" ::: "memory")
#define WAIT_L(n) asm volatile("s_waitcnt lgkmcnt(" #n ")" ::: "memory")
#define BAR() __builtin_amdgcn_s_barrier()
#define CFENCE() asm volatile("" ::: "memory")
template <int N> DI void waitv() { asm volatile("s_waitcnt vmcnt(%0)" ::"i"(N) : "memory"); }

DI float red_max_g(float x) {
  u32 u = __float_as_uint(x);
  auto r = __builtin_amdgcn_permlane16_swap(u, u, false, false);
  x = fmaxf(__uint_as_float(r[0]), __uint_as_float(r[1]));
  u = __float_as_uint(x);
  auto r2 = __builtin_amdgcn_permlane32_swap(u, u, false, false);
  return fmaxf(__uint_as_float(r2[0]), __uint_as_float(r2[1]));
}
DI float red_sum_g(float x) {
  u32 u = __float_as_uint(x);
  auto r = __builtin_amdgcn_permlane16_swap(u, u, false, false);
  x = __uint_as_float(r[0]) + __uint_as_float(r[1]);
  u = __float_as_uint(x);
  auto r2 = __builtin_amdgcn_permlane32_swap(u, u, false, false);
  return __uint_as_float(r2[0]) + __uint_as_float(r2[1]);
}

DI void dual_softmax(const f32x4 (&pacc)[2], int g, u32x4& pos, u32x4& neg) {
  constexpr float L2E = 1.4426950408889634f;
  float p[8];
#pragma unroll
  for (int i = 0; i < 4; i += 2) {
    u32 a = pk2(pacc[0][i], pacc[0][i + 1]); p[i] = bflo(a); p[i + 1] = bfhi(a);
    u32 b = pk2(pacc[1][i], pacc[1][i + 1]); p[4 + i] = bflo(b); p[4 + i + 1] = bfhi(b);
  }
  const bool v1 = g < 3;
  float mx = fmaxf(fmaxf(p[0], p[1]), fmaxf(p[2], p[3]));
  float mn = fminf(fminf(p[0], p[1]), fminf(p[2], p[3]));
  float mx1 = fmaxf(fmaxf(p[4], p[5]), fmaxf(p[6], p[7]));
  float mn1 = fminf(fminf(p[4], p[5]), fminf(p[6], p[7]));
  mx = v1 ? fmaxf(mx, mx1) : mx;
  mn = v1 ? fminf(mn, mn1) : mn;
  mx = red_max_g(mx);
  mn = -red_max_g(-mn);
  float ep[8], en[8];
  float sp = 0.f, sn = 0.f;
  const float mxl = mx * L2E, mnl = mn * L2E;
#pragma unroll
  for (int i = 0; i < 8; ++i) {
    float a = __builtin_amdgcn_exp2f(p[i] * L2E - mxl);
    float b = __builtin_amdgcn_exp2f(mnl - p[i] * L2E);
    if (i >= 4) { a = v1 ? a : 0.f; b = v1 ? b : 0.f; }
    ep[i] = a; en[i] = b; sp += a; sn += b;
  }
  sp = red_sum_g(sp); sn = red_sum_g(sn);
  const float rp = __builtin_amdgcn_rcpf(sp), rn = __builtin_amdgcn_rcpf(sn);
#pragma unroll
  for (int i = 0; i < 4; ++i) {
    pos[i] = pk2(ep[2 * i] * rp, ep[2 * i + 1] * rp);
    neg[i] = pk2(en[2 * i] * rn, en[2 * i + 1] * rn);
  }
}

constexpr int NXS = 2, NWS = 4;
constexpr int XSLOT = 16384, WSLOT = 12288;
constexpr int LDS_X = 0, LDS_W = NXS * XSLOT;
constexpr int LDS_A_BYTES = NXS * XSLOT + NWS * WSLOT;

DI void roleA(const u16* __restrict__ x, const u16* __restrict__ wqkv, const u16* __restrict__ wphi, char* __restrict__ ws, int h, int tile, char* lds) {
  const int tid = threadIdx.x, lane = tid & 63, w = __builtin_amdgcn_readfirstlane(tid >> 6);
  const int g = lane >> 4, l15 = lane & 15;
  const int tok0 = tile * 128 + w * 32;
  const u16* xsrc[4];
#pragma unroll
  for (int i = 0; i < 4; ++i) {
    int row = 8 * i + (lane >> 3); int cs = (lane & 7) ^ ((row >> 1) & 7);
    xsrc[i] = x + (size_t)(tok0 + row) * D + cs * 8;
  }
  const u16* wsrc;
  {
    int row = 16 * w + (lane >> 2); int cs = (lane & 3) ^ (2 * ((row >> 3) & 1));
    int coff = cs < 3 ? cs * 8 : 20;
    wsrc = wqkv + (size_t)row * (3 * D) + h * DH + coff;
  }
  auto issueX = [&](int c) {
    char* dst = lds + LDS_X + (c % NXS) * XSLOT + w * 4096;
#pragma unroll
    for (int i = 0; i < 4; ++i) glds16(xsrc[i] + c * 64, dst + i * 1024);
  };
  auto issueW = [&](int c) {
    char* dst = lds + LDS_W + (c % NWS) * WSLOT + w * 1024;
#pragma unroll
    for (int r = 0; r < 3; ++r) glds16(wsrc + (size_t)c * 64 * (3 * D) + r * D, dst + r * 4096);
  };
  u32 wraw[16];
  {
    const u16* wp = wphi + h * DH * DH;
#pragma unroll
    for (int ft = 0; ft < 2; ++ft) {
      int f = 16 * ft + l15; f = f < DH ? f : DH - 1;
#pragma unroll
      for (int j = 0; j < 8; ++j) {
        int rho = j < 4 ? 4 * g + j : 16 + 4 * g + j - 4;
        int d = rho < 24 ? rho : rho - 4;
        wraw[ft * 8 + j] = wp[d * DH + f];
      }
    }
  }
  __builtin_amdgcn_sched_barrier(0);
  issueW(0); issueX(0); issueW(1); issueX(1); issueW(2); issueW(3);
  __builtin_amdgcn_sched_barrier(0);
  f32x4 aq[2][2], ak[2][2], av[2][2];
#pragma unroll
  for (int a = 0; a < 2; ++a)
#pragma unroll
    for (int b = 0; b < 2; ++b) { aq[a][b] = (f32x4){0, 0, 0, 0}; ak[a][b] = aq[a][b]; av[a][b] = aq[a][b]; }
  u32 xoff[2][2];
#pragma unroll
  for (int rt = 0; rt < 2; ++rt)
#pragma unroll
    for (int ks = 0; ks < 2; ++ks) { int row = 16 * rt + l15; xoff[rt][ks] = w * 4096 + row * 128 + (((4 * ks + g) ^ ((row >> 1) & 7)) * 16); }
  u32 woff[2];
  {
    int q4 = l15 >> 2, p = lane & 3;
#pragma unroll
    for (int ct = 0; ct < 2; ++ct) woff[ct] = (8 * g + q4) * 64 + ((ct ^ (g & 1)) * 32) + p * 8;
  }
#pragma unroll
  for (int c = 0; c < 7; ++c) {
    if (c == 0) waitv<3 + 4 + 3 + 3>();
    else if (c == 1) waitv<3 + 3 + 4>();
    else if (c == 2) waitv<3 + 4>();
    else if (c == 3) waitv<3 + 4>();
    else if (c == 4) waitv<3 + 4>();
    else if (c == 5) waitv<4>();
    else waitv<0>();
    BAR();
    if (c >= 1 && c + 3 < 7) issueW(c + 3);
    const char* xs = lds + LDS_X + (c % NXS) * XSLOT;
    const char* wsb = lds + LDS_W + (c % NWS) * WSLOT;
    u32x4 xf[2][2], wf[3][2][2];
#pragma unroll
    for (int rt = 0; rt < 2; ++rt)
#pragma unroll
      for (int ks = 0; ks < 2; ++ks) xf[rt][ks] = *(const u32x4*)(xs + xoff[rt][ks]);
#pragma unroll
    for (int r = 0; r < 3; ++r)
#pragma unroll
      for (int ct = 0; ct < 2; ++ct)
#pragma unroll
        for (int ks = 0; ks < 2; ++ks) {
          u32x2 a = trread(wsb, woff[ct] + r * 4096 + ks * 2048);
          u32x2 b = trread(wsb, woff[ct] + r * 4096 + ks * 2048 + 256);
          wf[r][ct][ks] = (u32x4){a[0], a[1], b[0], b[1]};
        }
    WAIT_L(0);
    __builtin_amdgcn_sched_barrier(0);
    if (c + 2 < 7) issueX(c + 2);
#pragma unroll
    for (int ks = 0; ks < 2; ++ks)
#pragma unroll
      for (int ct = 0; ct < 2; ++ct)
#pragma unroll
        for (int rt = 0; rt < 2; ++rt) {
          aq[ct][rt] = mfma16(wf[0][ct][ks], xf[rt][ks], aq[ct][rt]);
          ak[ct][rt] = mfma16(wf[1][ct][ks], xf[rt][ks], ak[ct][rt]);
          av[ct][rt] = mfma16(xf[rt][ks], wf[2][ct][ks], av[ct][rt]);
        }
  }
  BAR();
  __builtin_amdgcn_sched_barrier(0);
  u32x4 wph[2];
#pragma unroll
  for (int ft = 0; ft < 2; ++ft) {
    const bool fok = 16 * ft + l15 < DH;
#pragma unroll
    for (int j = 0; j < 4; ++j) {
      const bool ok = fok && !(j >= 2 && g == 2);
      u32 v = wraw[ft * 8 + 2 * j] | (wraw[ft * 8 + 2 * j + 1] << 16);
      wph[ft][j] = ok ? v : 0u;
    }
  }
  char* phk = lds; char* vt = lds + 16384;
#pragma unroll
  for (int rt = 0; rt < 2; ++rt) {
    const int tl = w * 32 + rt * 16 + l15;
    {
      u32x4 kb = {pk2(ak[0][rt][0], ak[0][rt][1]), pk2(ak[0][rt][2], ak[0][rt][3]), pk2(ak[1][rt][0], ak[1][rt][1]), pk2(ak[1][rt][2], ak[1][rt][3])};
      f32x4 z4 = {0, 0, 0, 0}; f32x4 pk[2];
      pk[0] = mfma16(wph[0], kb, z4); pk[1] = mfma16(wph[1], kb, z4);
      u32x4 pos, neg; dual_softmax(pk, g, pos, neg);
      const int sw = ((tl >> 1) & 1) | (((tl >> 3) & 1) << 1);
      char* rowp = phk + tl * 128 + g * 8;
      *(u32x2*)(rowp + ((0 ^ sw) * 32)) = (u32x2){pos[0], pos[1]};
      *(u32x2*)(rowp + ((1 ^ sw) * 32)) = (u32x2){pos[2], pos[3]};
      *(u32x2*)(rowp + ((2 ^ sw) * 32)) = (u32x2){neg[0], neg[1]};
      *(u32x2*)(rowp + ((3 ^ sw) * 32)) = (u32x2){neg[2], neg[3]};
    }
#pragma unroll
    for (int ct = 0; ct < 2; ++ct) {
      f32x4 v = av[ct][rt];
      if (ct == 1 && l15 == 8) v = (f32x4){1.f, 1.f, 1.f, 1.f};
      *(u32x2*)(vt + (16 * ct + l15) * 272 + (w * 32 + rt * 16 + 4 * g) * 2) = (u32x2){pk2(v[0], v[1]), pk2(v[2], v[3])};
    }
  }
  __syncthreads();
  {
    f32x4 acc[2] = {{0, 0, 0, 0}, {0, 0, 0, 0}};
    const int q4 = l15 >> 2, p = lane & 3;
#pragma unroll
    for (int ks = 0; ks < 4; ++ks) {
      u32x2 a[2];
#pragma unroll
      for (int t = 0; t < 2; ++t) {
        int row = 32 * ks + 8 * g + 4 * t + q4;
        int sw = ((row >> 1) & 1) | (((row >> 3) & 1) << 1);
        a[t] = trread(phk, row * 128 + ((w ^ sw) * 32) + p * 8);
      }
      u32x4 af = {a[0][0], a[0][1], a[1][0], a[1][1]};
#pragma unroll
      for (int dt = 0; dt < 2; ++dt) {
        u32x4 bf = *(const u32x4*)(vt + (16 * dt + l15) * 272 + (32 * ks + 8 * g) * 2);
        acc[dt] = mfma16(af, bf, acc[dt]);
      }
    }
    f32x4* kvp = (f32x4*)(ws + WS_KVP) + ((size_t)(h * 16 + tile) * 8 + w * 2) * 64 + lane;
    kvp[0] = acc[0]; kvp[64] = acc[1];
  }
#pragma unroll
  for (int rt = 0; rt < 2; ++rt) {
    u32x4 qb = {pk2(aq[0][rt][0], aq[0][rt][1]), pk2(aq[0][rt][2], aq[0][rt][3]), pk2(aq[1][rt][0], aq[1][rt][1]), pk2(aq[1][rt][2], aq[1][rt][3])};
    f32x4 z4 = {0, 0, 0, 0}; f32x4 pq[2];
    pq[0] = mfma16(wph[0], qb, z4); pq[1] = mfma16(wph[1], qb, z4);
    u32x4 pos, neg; dual_softmax(pq, g, pos, neg);
    u32x4* dst = (u32x4*)(ws + WS_PHIQ) + ((size_t)(h * 128 + tile * 8 + w * 2 + rt) * 2) * 64 + lane;
    dst[0] = pos; dst[64] = neg;
  }
}

struct cf { float re, im; };
DI f32x2 cmul(f32x2 a, f32x2 b) {
  f32x2 m, r;
  asm("v_pk_mul_f32 %0, %1, %2 op_sel:[1,0] op_sel_hi:[1,1]" : "=v"(m) : "v"(b), "v"(a));
  asm("v_pk_fma_f32 %0, %1, %2, %3 op_sel:[0,0,1] op_sel_hi:[1,0,0] neg_lo:[0,0,1]" : "=v"(r) : "v"(a), "v"(b), "v"(m));
  return r;
}
DI f32x2 cexp_pow(float mr, float pr, float a) {
  float m = __builtin_amdgcn_exp2f(mr * a);
  float t = __builtin_amdgcn_fractf(pr * a);
  return (f32x2){m * __builtin_amdgcn_cosf(t), m * __builtin_amdgcn_sinf(t)};
}
constexpr int KG_ROW = 272;
constexpr int KG_SLAB = 32 * KG_ROW;

DI void kgen_wave(const u16* __restrict__ Are, const u16* __restrict__ Aim, const u16* __restrict__ Bre, const u16* __restrict__ Bim,
                  const u16* __restrict__ Cre, const u16* __restrict__ Cim, float dt, int d, int half, char* slab, f32x4 (&acc)[3]) {
  const int lane = threadIdx.x & 63, g = lane >> 4, l15 = lane & 15;
  const int base = d * NM + half * 128 + 2 * lane;
  const u32 are = *(const u32*)(Are + base), aim = *(const u32*)(Aim + base), bre = *(const u32*)(Bre + base), bim = *(const u32*)(Bim + base),
            cre = *(const u32*)(Cre + base), cim = *(const u32*)(Cim + base);
#pragma unroll
  for (int m = 0; m < 3; ++m) acc[m] = (f32x4){0, 0, 0, 0};
#pragma unroll
  for (int ps = 0; ps < 2; ++ps) {
    const float ar = dt * (ps ? bfhi(are) : bflo(are)), ai = dt * (ps ? bfhi(aim) : bflo(aim));
    const float br = dt * (ps ? bfhi(bre) : bflo(bre)), bi = dt * (ps ? bfhi(bim) : bflo(bim));
    const float cr = ps ? bfhi(cre) : bflo(cre), ci = ps ? bfhi(cim) : bflo(cim);
    const float mr = ar * 1.4426950408889634f, pr = ai * 0.15915494309189535f;
    f32x2 W = {cr * br - ci * bi, -(cr * bi + ci * br)};
    f32x2 z1c = cexp_pow(mr, -pr, 1.f);
    f32x2 z8 = cexp_pow(mr, pr, 8.f);
    f32x2 a128 = cexp_pow(mr, -pr, 128.f);
    u32* wr = (u32*)slab + lane;
    {
      f32x2 y = {1.f, 0.f};
      wr[0] = pk2(1.f, 0.f);
      y = z8; wr[68] = pk2(y.x, y.y);
#pragma unroll
      for (int c = 2; c < 16; ++c) { y = cmul(y, z8); wr[c * 68] = pk2(y.x, y.y); }
    }
    f32x2 R0[8];
    R0[0] = W;
#pragma unroll
    for (int b = 1; b < 8; ++b) R0[b] = cmul(R0[b - 1], z1c);
    f32x2 A[17];
    A[1] = a128;
#pragma unroll
    for (int m = 2; m <= 16; ++m) A[m] = cmul(A[m - 1], a128);
    u32* xr = wr + 16 * 68;
#pragma unroll
    for (int b = 0; b < 8; ++b) { xr[b * 68] = pk2(R0[b].x, R0[b].y); f32x2 t = cmul(R0[b], A[1]); xr[(8 + b) * 68] = pk2(t.x, t.y); }
    u32x4 yf[4];
    CFENCE();
    {
      const char* rd = slab + l15 * KG_ROW + g * 16;
#pragma unroll
      for (int ks = 0; ks < 4; ++ks) yf[ks] = *(const u32x4*)(rd + ks * 64);
#pragma unroll
      for (int ks = 0; ks < 4; ++ks) { u32x4 xf = *(const u32x4*)(rd + 16 * KG_ROW + ks * 64); acc[0] = mfma16(xf, yf[ks], acc[0]); }
    }
    CFENCE();
#pragma unroll
    for (int hh = 0; hh < 2; ++hh)
#pragma unroll
      for (int b = 0; b < 4; ++b) { f32x2 t = cmul(R0[2 * b], A[2 + hh]); xr[(b + 4 * hh) * 68] = pk2(t.x, t.y); }
#pragma unroll
    for (int hh = 0; hh < 4; ++hh)
#pragma unroll
      for (int b = 0; b < 2; ++b) { f32x2 t = cmul(R0[4 * b], A[4 + hh]); xr[(8 + b + 2 * hh) * 68] = pk2(t.x, t.y); }
    CFENCE();
    {
      const char* rd = slab + (16 + l15) * KG_ROW + g * 16;
#pragma unroll
      for (int ks = 0; ks < 4; ++ks) { u32x4 xf = *(const u32x4*)(rd + ks * 64); acc[1] = mfma16(xf, yf[ks], acc[1]); }
    }
    CFENCE();
#pragma unroll
    for (int hh = 0; hh < 9; ++hh) { f32x2 t = cmul(W, A[8 + hh]); xr[hh * 68] = pk2(t.x, t.y); }
    CFENCE();
    {
      const char* rd = slab + (16 + l15) * KG_ROW + g * 16;
#pragma unroll
      for (int ks = 0; ks < 4; ++ks) { u32x4 xf = *(const u32x4*)(rd + ks * 64); acc[2] = mfma16(xf, yf[ks], acc[2]); }
    }
    CFENCE();
  }
}
DI int kv_index(int j) {
  u32 u = __float_as_uint((float)j);
  u += 0x7fffu + ((u >> 16) & 1u); u &= 0xffff0000u;
  const int v = (int)__uint_as_float(u);
  const int c = (v >> 3) & 15;
  int e = (int)(u >> 23) - 127 - 7; e = e > 0 ? e : 0;
  const int b = (v & 7) >> e;
  const int hh = (v >> 7) - (e ? (1 << e) : 0);
  const int row = (e ? 8 + 8 * e : 0) + b + (8 >> e) * hh;
  return row * 16 + c;
}

DI void roleB1(const u16* const* in, char* __restrict__ ws, int bb, char* lds) {
  const int tid = threadIdx.x, lane = tid & 63, w = __builtin_amdgcn_readfirstlane(tid >> 6);
  const int g = lane >> 4, l15 = lane & 15;
  const u16* x = in[0];
  {
    const int tt = bb / 7, cg = bb % 7;
    char* img = lds + 40960;
#pragma unroll
    for (int i = 0; i < 2; ++i) {
      int row = (tid >> 3) + 32 * i, ch = tid & 7;
      u32x4 v = *(const u32x4*)(x + (size_t)(tt * 64 + row) * D + cg * 64 + ch * 8);
      u32* dst = (u32*)(img + row * 132 + ch * 16);
      dst[0] = v[0]; dst[1] = v[1]; dst[2] = v[2]; dst[3] = v[3];
    }
    __syncthreads();
#pragma unroll
    for (int i = 0; i < 2; ++i) {
      int ch = (tid >> 3) + 32 * i, oc = tid & 7;
      u32 o[4];
#pragma unroll
      for (int j = 0; j < 4; ++j) {
        u32 lo = *(const u16*)(img + (8 * oc + 2 * j) * 132 + ch * 2);
        u32 hi = *(const u16*)(img + (8 * oc + 2 * j + 1) * 132 + ch * 2);
        o[j] = lo | (hi << 16);
      }
      *(u32x4*)((u16*)(ws + WS_XT) + (size_t)(cg * 64 + ch) * S + tt * 64 + oc * 8) = (u32x4){o[0], o[1], o[2], o[3]};
    }
  }
  if (w < 2) {
    int fr = bb + 224 * w;
    if (fr < 392) {
      int ct = fr / 14, ks = fr % 14;
      const u16* wo = in[3];
      u32 o[4];
#pragma unroll
      for (int j = 0; j < 4; ++j) {
        u32 lo = wo[(size_t)(32 * ks + 8 * g + 2 * j) * D + 16 * ct + l15];
        u32 hi = wo[(size_t)(32 * ks + 8 * g + 2 * j + 1) * D + 16 * ct + l15];
        o[j] = lo | (hi << 16);
      }
      ((u32x4*)(ws + WS_WOF))[(size_t)fr * 64 + lane] = (u32x4){o[0], o[1], o[2], o[3]};
    }
  }
  if (w >= 2) {
    const u16* gw = in[13];
    u32x4 gv = {0, 0, 0, 0};
    if (lane < 56) gv = *(const u32x4*)(gw + lane * 8);
#pragma unroll
    for (int i = 0; i < 5; ++i) {
      int tok = bb * 10 + (w - 2) * 5 + i;
      if (tok < S) {
        float s = 0.f;
        if (lane < 56) {
          u32x4 xv = *(const u32x4*)(x + (size_t)tok * D + lane * 8);
#pragma unroll
          for (int j = 0; j < 4; ++j) {
            u32 pr = pk2(bflo(xv[j]) * bflo(gv[j]), bfhi(xv[j]) * bfhi(gv[j]));
            s += bflo(pr) + bfhi(pr);
          }
        }
#pragma unroll
        for (int m = 32; m >= 1; m >>= 1) s += __shfl_xor(s, m);
        float gg = rbf(s);
        gg = rbf(1.f / (1.f + __expf(-gg)));
        if (lane == 0) ((float*)(ws + WS_G))[tok] = gg;
      }
    }
  }
  const int d = bb * 2 + (w >> 1);
  const float dt = rbf(expf(bf2f(in[11][d])));
  f32x4 acc[3];
  kgen_wave(in[5], in[6], in[7], in[8], in[9], in[10], dt, d, w & 1, lds + w * KG_SLAB, acc);
  float* kt = (float*)(lds + 53248) + w * 768;
#pragma unroll
  for (int m = 0; m < 3; ++m)
#pragma unroll
    for (int i = 0; i < 4; ++i) kt[(16 * m + 4 * g + i) * 16 + l15] = acc[m][i];
  __syncthreads();
  {
    const int ch = tid >> 7, t7 = tid & 127;
    const float* k0 = (const float*)(lds + 53248) + (2 * ch) * 768; const float* k1 = k0 + 768;
    u16* kr = (u16*)(ws + WS_KR) + (size_t)(bb * 2 + ch) * 2304;
#pragma unroll
    for (int i = 0; i < 2; ++i) {
      int c8 = t7 + 128 * i;
      int s = c8 >> 1, t0 = (c8 & 1) * 8;
      float v[8];
#pragma unroll
      for (int j = 0; j < 8; ++j) { int idx = kv_index(16 * s + 15 - (t0 + j)); v[j] = k0[idx] + k1[idx]; }
      *(u32x4*)(kr + 256 + c8 * 8) = (u32x4){pk2(v[0], v[1]), pk2(v[2], v[3]), pk2(v[4], v[5]), pk2(v[6], v[7])};
    }
    if (t7 < 32) *(u32x4*)(kr + t7 * 8) = (u32x4){0, 0, 0, 0};
  }
}

struct Params { const u16* in[14]; char* ws; u16* out; };

__global__ __launch_bounds__(256, 2) void k1(Params p) {
  __shared__ __attribute__((aligned(16))) char lds[LDS_A_BYTES];
  const int b = blockIdx.x;
  if (b < 256) {
    const int xcd = b & 7, j = b >> 3;
    const int h = 8 * (xcd & 1) + (j & 7), tile = 4 * (xcd >> 1) + (j >> 3);
    roleA(p.in[0], p.in[1], p.in[2], p.ws, h, tile, lds);
  } else {
    roleB1(p.in, p.ws, b - 256, lds);
  }
}

DI void roleO(char* __restrict__ ws, int h, int tile, char* lds) {
  const int tid = threadIdx.x, lane = tid & 63, w = __builtin_amdgcn_readfirstlane(tid >> 6);
  const int g = lane >> 4, l15 = lane & 15;
  u32x4 pq[2][2];
#pragma unroll
  for (int rt = 0; rt < 2; ++rt) {
    const u32x4* src = (const u32x4*)(ws + WS_PHIQ) + ((size_t)(h * 128 + tile * 8 + w * 2 + rt) * 2) * 64 + lane;
    pq[rt][0] = src[0]; pq[rt][1] = src[64];
  }
  f32x4 s0 = {0, 0, 0, 0}, s1 = s0;
  const f32x4* kvp = (const f32x4*)(ws + WS_KVP) + ((size_t)(h * 16) * 8 + w * 2) * 64 + lane;
#pragma unroll
  for (int pI = 0; pI < 16; ++pI) { s0 += kvp[(size_t)pI * 512]; s1 += kvp[(size_t)pI * 512 + 64]; }
  u32x2* kvb = (u32x2*)lds;
  kvb[(w * 2 + 0) * 64 + lane] = (u32x2){pk2(s0[0], s0[1]), pk2(s0[2], s0[3])};
  kvb[(w * 2 + 1) * 64 + lane] = (u32x2){pk2(s1[0], s1[1]), pk2(s1[2], s1[3])};
  __syncthreads();
  u32x4 kf[2][2];
#pragma unroll
  for (int dt = 0; dt < 2; ++dt)
#pragma unroll
    for (int sg = 0; sg < 2; ++sg) {
      u32x2 a = kvb[((2 * sg) * 2 + dt) * 64 + lane], b = kvb[((2 * sg + 1) * 2 + dt) * 64 + lane];
      kf[dt][sg] = (u32x4){a[0], a[1], b[0], b[1]};
    }
#pragma unroll
  for (int rt = 0; rt < 2; ++rt) {
    f32x4 o[2];
#pragma unroll
    for (int dt = 0; dt < 2; ++dt) {
      o[dt] = mfma16(kf[dt][0], pq[rt][0], (f32x4){0, 0, 0, 0});
      o[dt] = mfma16(kf[dt][1], pq[rt][1], o[dt]);
    }
    float den = __shfl(o[1][0], 32 + l15);
    den = rbf(rbf(den) + 1e-6f);
    const float rd = 1.0f / den;
    const int tok = tile * 128 + w * 32 + rt * 16 + l15;
    u16* op = (u16*)(ws + WS_O) + (size_t)tok * D + h * DH;
#pragma unroll
    for (int dt = 0; dt < 2; ++dt) {
      float v[4];
#pragma unroll
      for (int i = 0; i < 4; ++i) v[i] = rbf(o[dt][i]) / den;
      u32x2 r = {pk2(v[0], v[1]), pk2(v[2], v[3])};
      if (dt == 0) *(u32x2*)(op + 4 * g) = r;
      else if (g < 2) *(u32x2*)(op + 16 + 4 * g) = r;
      else if (g == 3) *(u32x2*)(op + 24) = r;
    }
    (void)rd;
  }
}

constexpr int UC_STRIDE = 2112 * 2;
constexpr int UCOPY = 4160;
constexpr int UCH = 4 * UCOPY;
DI void roleConv(char* __restrict__ ws, int bb, char* lds) {
  const int tid = threadIdx.x, lane = tid & 63, w = __builtin_amdgcn_readfirstlane(tid >> 6);
  const int g = lane >> 4, l15 = lane & 15;
  char* uc = lds; char* kr = lds + 2 * UCH; char* xch = kr + 2 * 4608;
  {
    const int ch = tid >> 7, t7 = tid & 127;
    const u16* xt = (const u16*)(ws + WS_XT) + (size_t)(bb * 2 + ch) * S;
    const u16* krg = (const u16*)(ws + WS_KR) + (size_t)(bb * 2 + ch) * 2304;
    for (int i = t7; i < 288; i += 128) *(u32x4*)(kr + ch * 4608 + i * 16) = *(const u32x4*)(krg + i * 8);
#pragma unroll
    for (int i = 0; i < 2; ++i) {
      int q = t7 + 128 * i;
      u32x4 a = *(const u32x4*)(xt + q * 8);
      u32x4 b = {0, 0, 0, 0};
      if (q < 255) b = *(const u32x4*)(xt + q * 8 + 8);
      u32 e[6] = {a[0], a[1], a[2], a[3], b[0], b[1]};
      char* base = uc + ch * UCH + (16 + q * 8) * 2;
      *(u32x4*)(base) = (u32x4){e[0], e[1], e[2], e[3]};
      *(u32x4*)(base + UCOPY) = (u32x4){__builtin_amdgcn_alignbit(e[1], e[0], 16), __builtin_amdgcn_alignbit(e[2], e[1], 16), __builtin_amdgcn_alignbit(e[3], e[2], 16), __builtin_amdgcn_alignbit(e[4], e[3], 16)};
      *(u32x4*)(base + 2 * UCOPY) = (u32x4){e[1], e[2], e[3], e[4]};
      *(u32x4*)(base + 3 * UCOPY) = (u32x4){__builtin_amdgcn_alignbit(e[2], e[1], 16), __builtin_amdgcn_alignbit(e[3], e[2], 16), __builtin_amdgcn_alignbit(e[4], e[3], 16), __builtin_amdgcn_alignbit(e[5], e[4], 16)};
    }
    if (t7 < 4) {
      int c = t7;
      u16* cp = (u16*)(uc + ch * UCH + c * UCOPY);
      for (int i = 0; i < 16; ++i) { int m = i + c - 16; cp[i] = m >= 0 ? xt[m] : (u16)0; }
    }
  }
  __syncthreads();
  const int ch = w >> 1, rh = w & 1;
  const int idx0 = 8 * g + l15 + 1;
  const char* abase = uc + ch * UCH + (idx0 & 3) * UCOPY + (idx0 & ~3) * 2;
  const char* bbase = kr + ch * 4608 + (256 + 16 * (l15 - (g >> 1)) + 8 * (g & 1)) * 2;
  f32x4 acc[8];
#pragma unroll
  for (int i = 0; i < 8; ++i) acc[i] = (f32x4){0, 0, 0, 0};
#pragma unroll
  for (int rr = 0; rr < 4; ++rr) {
    const int r = rh * 4 + rr;
    u32x4 af[8], bf[8];
#pragma unroll
    for (int a = 0; a < 8; ++a) {
      const char* pa = abase + (256 * a + 32 * r) * 2;
      u32x2 lo = *(const u32x2*)pa, hi = *(const u32x2*)(pa + 8);
      af[a] = (u32x4){lo[0], lo[1], hi[0], hi[1]};
      bf[a] = *(const u32x4*)(bbase + (256 * a - 32 * r) * 2);
    }
#pragma unroll
    for (int a = 0; a < 8; ++a)
#pragma unroll
      for (int b = 0; b + a < 8; ++b) acc[a + b] = mfma16(af[a], bf[b], acc[a + b]);
  }
  f32x4* xw = (f32x4*)xch + (size_t)w * 256 + lane;
#pragma unroll
  for (int i = 0; i < 4; ++i) {
    f32x4 t;
#pragma unroll
    for (int e = 0; e < 4; ++e) t[e] = rh ? acc[i][e] : acc[4 + i][e];
    xw[i * 64] = t;
  }
  __syncthreads();
  const f32x4* xr = (const f32x4*)xch + (size_t)(w ^ 1) * 256 + lane;
  u16* yt = (u16*)(ws + WS_YT) + (size_t)(bb * 2 + ch) * S;
#pragma unroll
  for (int i = 0; i < 4; ++i) {
    f32x4 v = xr[i * 64];
#pragma unroll
    for (int e = 0; e < 4; ++e) v[e] += rh ? acc[4 + i][e] : acc[i][e];
    int pt = rh * 4 + i;
    *(u32x2*)(yt + 256 * pt + 16 * l15 + 4 * g) = (u32x2){pk2(v[0], v[1]), pk2(v[2], v[3])};
  }
}

__global__ __launch_bounds__(256, 2) void k2(Params p) {
  __shared__ __attribute__((aligned(16))) char lds[61440];
  const int b = blockIdx.x;
  if (b < 224) roleConv(p.ws, b, lds);
  else {
    const int bb = b - 224;
    roleO(p.ws, bb >> 4, bb & 15, lds);
  }
}

__global__ __launch_bounds__(256, 2) void k3(Params p) {
  __shared__ __attribute__((aligned(16))) char lds[57344 + 8192 + 8192 + 8192];
  const int tid = threadIdx.x, lane = tid & 63, w = __builtin_amdgcn_readfirstlane(tid >> 6);
  const int g = lane >> 4, l15 = lane & 15;
  const int tt = blockIdx.x / 7, cg = blockIdx.x % 7;
  const int tok0 = tt * 64, c0 = cg * 64;
  char* ol = lds;
  char* yl = lds + 57344;
  char* xl = yl + 8192;
  char* outl = xl + 8192;
  const u16* o = (const u16*)(p.ws + WS_O);
#pragma unroll
  for (int c = 0; c < 7; ++c)
#pragma unroll
    for (int i = 0; i < 2; ++i) {
      int row = 16 * w + 8 * i + (lane >> 3); int cs = (lane & 7) ^ ((row >> 1) & 7);
      glds16(o + (size_t)(tok0 + row) * D + c * 64 + cs * 8, ol + c * 8192 + (16 * w + 8 * i) * 128);
    }
#pragma unroll
  for (int i = 0; i < 2; ++i) {
    int row = 16 * w + 8 * i + (lane >> 3);
    glds16((const u16*)(p.ws + WS_YT) + (size_t)(c0 + row) * S + tok0 + (lane & 7) * 8, yl + (16 * w + 8 * i) * 128);
    int cs = (lane & 7) ^ (row & 7);
    glds16(p.in[0] + (size_t)(tok0 + row) * D + c0 + cs * 8, xl + (16 * w + 8 * i) * 128);
  }
  u32x4 wf[14];
  {
    const u32x4* src = (const u32x4*)(p.ws + WS_WOF) + (size_t)(4 * cg + w) * 14 * 64 + lane;
#pragma unroll
    for (int ks = 0; ks < 14; ++ks) wf[ks] = src[ks * 64];
  }
  float gate[4];
#pragma unroll
  for (int rt = 0; rt < 4; ++rt) gate[rt] = ((const float*)(p.ws + WS_G))[tok0 + 16 * rt + l15];
  u32x2 dsk = *(const u32x2*)(p.in[12] + c0 + 16 * w + 4 * g);
  WAIT_V(0);
  __syncthreads();
  f32x4 acc[4];
#pragma unroll
  for (int rt = 0; rt < 4; ++rt) acc[rt] = (f32x4){0, 0, 0, 0};
#pragma unroll
  for (int ks = 0; ks < 14; ++ks)
#pragma unroll
    for (int rt = 0; rt < 4; ++rt) {
      int row = 16 * rt + l15; int cc = 4 * (ks & 1) + g;
      u32x4 of = *(const u32x4*)(ol + (ks >> 1) * 8192 + row * 128 + ((cc ^ ((row >> 1) & 7)) * 16));
      acc[rt] = mfma16(wf[ks], of, acc[rt]);
    }
  const float ds[4] = {bflo(dsk[0]), bfhi(dsk[0]), bflo(dsk[1]), bfhi(dsk[1])};
  const int q4 = l15 >> 2, pp = lane & 3;
#pragma unroll
  for (int rt = 0; rt < 4; ++rt) {
    u32x2 yv = trread(yl, (16 * w + 4 * g + q4) * 128 + (16 * rt + 4 * pp) * 2);
    int tok = 16 * rt + l15;
    u32 xo = tok * 128 + (((2 * w + (g >> 1)) ^ (tok & 7)) * 16) + (g & 1) * 8;
    u32x2 xv = *(const u32x2*)(xl + xo);
    float y[4] = {bflo(yv[0]), bfhi(yv[0]), bflo(yv[1]), bfhi(yv[1])};
    float xx[4] = {bflo(xv[0]), bfhi(xv[0]), bflo(xv[1]), bfhi(xv[1])};
    const float gg = gate[rt], og = rbf(1.f - gg);
    float r[4];
#pragma unroll
    for (int i = 0; i < 4; ++i) {
      float a = rbf(acc[rt][i]);
      float b = rbf(y[i] + rbf(smul(xx[i], ds[i])));
      r[i] = rbf(gg * a) + rbf(og * b);
    }
    *(u32x2*)(outl + xo) = (u32x2){pk2(r[0], r[1]), pk2(r[2], r[3])};
  }
  __syncthreads();
#pragma unroll
  for (int i = 0; i < 2; ++i) {
    int row = (tid >> 3) + 32 * i, ch = tid & 7;
    u32x4 v = *(const u32x4*)(outl + row * 128 + ((ch ^ (row & 7)) * 16));
    *(u32x4*)(p.out + (size_t)(tok0 + row) * D + c0 + ch * 8) = v;
  }
}

extern "C" void kernel_launch(void* const* d_in, const int* in_sizes, int n_in, void* d_out, int out_size, void* d_ws, size_t ws_size, hipStream_t stream) {
  Params p;
  for (int i = 0; i < 14; ++i) p.in[i] = (const u16*)d_in[i];
  p.ws = (char*)d_ws; p.out = (u16*)d_out;
  k1<<<480, 256, 0, stream>>>(p);
  k2<<<480, 256, 0, stream>>>(p);
  k3<<<224, 256, 0, stream>>>(p);
}
```

```cpp
#include <hip/hip_runtime.h>
#include <stdint.h>

typedef unsigned short u16;
typedef unsigned int u32;
typedef short s16x4 __attribute__((ext_vector_type(4)));
typedef __bf16 bf16x8 __attribute__((ext_vector_type(8)));
typedef __bf16 bf16x2 __attribute__((ext_vector_type(2)));
typedef float f32x4 __attribute__((ext_vector_type(4)));
typedef float f32x2 __attribute__((ext_vector_type(2)));
typedef u32 u32x4 __attribute__((ext_vector_type(4)));
typedef u32 u32x2 __attribute__((ext_vector_type(2)));
typedef __amdgpu_buffer_rsrc_t rsrc_t;
#define LAS __attribute__((address_space(3)))
#define GAS __attribute__((address_space(1)))
#define DI __device__ __forceinline__

constexpr int S = 2048, D = 448, H = 16, DH = 28, NM = 256;

constexpr u32 WS_GEN = 0;
constexpr u32 WS_FLAG = 4096, FSTR = 128;
constexpr u32 F_XT = 0;
constexpr u32 F_Y = 1792;
constexpr u32 F_A = 3584;
constexpr u32 NFLAG = F_A + 256;
constexpr u32 WS_XT = 1u << 20;
constexpr u32 WS_KVP = WS_XT + D * S * 2;
constexpr u32 WS_OF = WS_KVP + 16 * 16 * 4 * 1024;
constexpr u32 WS_YT = WS_OF + 128 * 16 * 1024;
constexpr u32 WS_GATE = WS_YT + D * S * 2;
constexpr u32 WS_END = WS_GATE + S * 4;
static_assert(WS_FLAG + NFLAG * FSTR <= WS_XT, "flags");
constexpr int MAXSPIN = 400000;
#define CODE_BYTES 8192

#define STAMP(i)
#define STAMP_DECL
#define STAMP_FLUSH(ws)

DI float bflo(u32 v) { return __uint_as_float(v << 16); }
DI float bfhi(u32 v) { return __uint_as_float(v & 0xffff0000u); }
DI u32 pk2(float lo, float hi) { f32x2 p = {lo, hi}; bf16x2 q = __builtin_convertvector(p, bf16x2); return __builtin_bit_cast(u32, q); }
DI float smul(float a, float b) { float r; asm("v_mul_f32 %0, %1, %2" : "=v"(r) : "v"(a), "v"(b)); return r; }
DI float rbf(float f) { return bflo(pk2(f, f)); }
DI f32x4 mfma16(u32x4 a, u32x4 b, f32x4 c) {
  return __builtin_amdgcn_mfma_f32_16x16x32_bf16(__builtin_bit_cast(bf16x8, a), __builtin_bit_cast(bf16x8, b), c, 0, 0, 0);
}
DI u32x2 trread(const char* lds_base, u32 off) {
  s16x4 t = __builtin_amdgcn_ds_read_tr16_b64_v4i16((LAS s16x4*)((LAS char*)lds_base + off));
  return __builtin_bit_cast(u32x2, t);
}
DI rsrc_t mkrs(const void* p, u32 bytes) { return __builtin_amdgcn_make_buffer_rsrc((void*)p, 0, bytes, 0x00020000); }
DI void dma16(rsrc_t r, char* lds_dst, u32 voff, u32 soff) { __builtin_amdgcn_raw_ptr_buffer_load_lds(r, (LAS void*)lds_dst, 16, voff, soff, 0, 0); }
DI void touch4(rsrc_t r, char* lds_dst, u32 voff, u32 soff) { __builtin_amdgcn_raw_ptr_buffer_load_lds(r, (LAS void*)lds_dst, 4, voff, soff, 0, 0); }
DI u32x4 mkrs4(const void* p, u32 bytes) { unsigned long long a = (unsigned long long)p; return (u32x4){(u32)a, (u32)(a >> 32) & 0xffffu, bytes, 0x00020000u}; }
DI u32 ldsaddr(const char* p) { return (u32)(uintptr_t)(LAS const char*)p; }
DI void adma16(u32x4 rs, u32 lds_dst, u32 voff, u32 soff) {
  asm volatile("s_mov_b32 m0, %0\n\ts_nop 0\n\tbuffer_load_dwordx4 %1, %2, %3 offen lds" ::"s"(lds_dst), "v"(voff), "s"(rs), "s"(soff) : "memory");
}
DI void adma16h(u32x4 rs, u32 lds_dst, u32 voff, u32 soff) {
  asm volatile("s_mov_b32 m0, %0\n\ts_mov_b32 exec_hi, 0\n\tbuffer_load_dwordx4 %1, %2, %3 offen lds\n\ts_mov_b32 exec_hi, -1" ::"s"(lds_dst), "v"(voff), "s"(rs), "s"(soff) : "memory");
}
DI u32 sload(const void* p) { return *(const __attribute__((address_space(4))) u32*)(unsigned long long)p; }
DI void atouch4(u32x4 rs, u32 lds_dst, u32 voff, u32 soff) {
  asm volatile("s_mov_b32 m0, %0\n\ts_nop 0\n\tbuffer_load_dword %1, %2, %3 offen lds" ::"s"(lds_dst), "v"(voff), "s"(rs), "s"(soff) : "memory");
}
DI u32x4 ald16(u32x4 rs, u32 voff, u32 soff) {
  u32x4 r; asm volatile("buffer_load_dwordx4 %0, %1, %2, %3 offen" : "=v"(r) : "v"(voff), "s"(rs), "s"(soff) : "memory"); return r;
}
template <int N> DI void waitv4(u32x4& a, u32x4& b, u32x4& c, u32x4& d) { asm volatile("s_waitcnt vmcnt(%4)" : "+v"(a), "+v"(b), "+v"(c), "+v"(d) : "i"(N) : "memory"); }
DI u32x4 ld16(rsrc_t r, u32 voff, u32 soff) { return __builtin_amdgcn_raw_buffer_load_b128(r, voff, soff, 0); }
DI u32x4 ld16c(rsrc_t r, u32 voff, u32 soff) { return __builtin_amdgcn_raw_buffer_load_b128(r, voff, soff, 16); }
DI u32x2 ld8c(rsrc_t r, u32 voff, u32 soff) { return __builtin_amdgcn_raw_buffer_load_b64(r, voff, soff, 16); }
DI u32 ld4c(rsrc_t r, u32 voff, u32 soff) { return __builtin_amdgcn_raw_buffer_load_b32(r, voff, soff, 16); }
DI u32 ld2c(rsrc_t r, u32 voff, u32 soff) { return __builtin_amdgcn_raw_buffer_load_b16(r, voff, soff, 16); }
DI void st16c(u32x4 v, rsrc_t r, u32 voff, u32 soff) { __builtin_amdgcn_raw_buffer_store_b128(v, r, voff, soff, 16); }
DI void st4c(u32 v, rsrc_t r, u32 voff, u32 soff) { __builtin_amdgcn_raw_buffer_store_b32(v, r, voff, soff, 16); }
#define WAIT_L(n) asm volatile("s_waitcnt lgkmcnt(" #n ")" ::: "memory")
#define BAR() __builtin_amdgcn_s_barrier()
#define CFENCE() asm volatile("" ::: "memory")
template <int N> DI void waitv() { asm volatile("s_waitcnt vmcnt(%0)" ::"i"(N) : "memory"); }
DI void bar_l() { WAIT_L(0); BAR(); CFENCE(); }

DI u32 flag_off(u32 idx) { return WS_FLAG + idx * FSTR; }
DI void publish(char* ws, u32 idx, u32 tag, int lane) {
  waitv<0>();
  if (lane == 0) __hip_atomic_store((u32*)(ws + flag_off(idx)), tag, __ATOMIC_RELAXED, __HIP_MEMORY_SCOPE_AGENT);
}
DI void pollf(char* ws, u32 idx, u32 tag) {
  const u32* f = (const u32*)(ws + flag_off(idx));
#pragma unroll 1
  for (int it = 0; it < MAXSPIN; ++it) {
    u32 v = __hip_atomic_load(f, __ATOMIC_RELAXED, __HIP_MEMORY_SCOPE_AGENT);
    if (__all(v == tag)) break;
    __builtin_amdgcn_s_sleep(1);
  }
  CFENCE();
}

DI float red_max_g(float x) {
  u32 u = __float_as_uint(x);
  auto r = __builtin_amdgcn_permlane16_swap(u, u, false, false);
  x = fmaxf(__uint_as_float(r[0]), __uint_as_float(r[1]));
  u = __float_as_uint(x);
  auto r2 = __builtin_amdgcn_permlane32_swap(u, u, false, false);
  return fmaxf(__uint_as_float(r2[0]), __uint_as_float(r2[1]));
}
DI float red_sum_g(float x) {
  u32 u = __float_as_uint(x);
  auto r = __builtin_amdgcn_permlane16_swap(u, u, false, false);
  x = __uint_as_float(r[0]) + __uint_as_float(r[1]);
  u = __float_as_uint(x);
  auto r2 = __builtin_amdgcn_permlane32_swap(u, u, false, false);
  return __uint_as_float(r2[0]) + __uint_as_float(r2[1]);
}
template <int CTRL> DI float dpp_add(float x) {
  return x + __uint_as_float(__builtin_amdgcn_update_dpp(0, __float_as_uint(x), CTRL, 0xf, 0xf, false));
}
DI float red_sum_all(float x) {
  x = dpp_add<0x128>(x); x = dpp_add<0x124>(x); x = dpp_add<0x122>(x); x = dpp_add<0x121>(x);
  return red_sum_g(x);
}

#define APRIO 0
#define BPRIO 2
DI void dual_softmax(const f32x4 (&pacc)[2], u32x4& pos, u32x4& neg) {
  constexpr float L2E = 1.4426950408889634f;
  float p[7];
  {
    u32 a = pk2(pacc[0][0], pacc[0][1]), b = pk2(pacc[0][2], pacc[0][3]), c = pk2(pacc[1][0], pacc[1][1]), d = pk2(pacc[1][2], pacc[1][2]);
    p[0] = bflo(a); p[1] = bfhi(a); p[2] = bflo(b); p[3] = bfhi(b); p[4] = bflo(c); p[5] = bfhi(c); p[6] = bflo(d);
  }
  float mx = fmaxf(fmaxf(fmaxf(p[0], p[1]), fmaxf(p[2], p[3])), fmaxf(fmaxf(p[4], p[5]), p[6]));
  float mn = fminf(fminf(fminf(p[0], p[1]), fminf(p[2], p[3])), fminf(fminf(p[4], p[5]), p[6]));
  mx = red_max_g(mx);
  mn = -red_max_g(-mn);
  float ep[7], en[7];
  float sp = 0.f, sn = 0.f;
  const float mxl = mx * L2E, mnl = mn * L2E;
#pragma unroll
  for (int i = 0; i < 7; ++i) {
    ep[i] = __builtin_amdgcn_exp2f(__builtin_fmaf(p[i], L2E, -mxl));
    en[i] = __builtin_amdgcn_exp2f(__builtin_fmaf(p[i], -L2E, mnl));
    sp += ep[i]; sn += en[i];
  }
  sp = red_sum_g(sp); sn = red_sum_g(sn);
  const float rp = __builtin_amdgcn_rcpf(sp), rn = __builtin_amdgcn_rcpf(sn);
#pragma unroll
  for (int i = 0; i < 3; ++i) {
    pos[i] = pk2(ep[2 * i] * rp, ep[2 * i + 1] * rp);
    neg[i] = pk2(en[2 * i] * rn, en[2 * i + 1] * rn);
  }
  pos[3] = pk2(ep[6] * rp, 0.f); neg[3] = pk2(en[6] * rn, 0.f);
}
DI float acc_sel(u32 v, u32 sel, float a) { return __builtin_amdgcn_fdot2_f32_bf16(__builtin_bit_cast(bf16x2, v), __builtin_bit_cast(bf16x2, sel), a, false); }
DI bool tag_ok(u32 v3, u32 t16) { return __all((v3 >> 16) == t16); }

struct Params { const u16* in[14]; char* ws; u16* out; };

constexpr int A_X = 0, A_W = 32768, A_WPHI = 73728, A_GW = 75776, A_VT = 16384, A_Q6 = 24576, A_KVB = 76800, A_RED = 65536;
constexpr int LDS_BYTES = 81920;
constexpr int WROW = 3 * D * 2;
constexpr int WCH = 64 * WROW;

DI void roleA(const Params& p, int x8, int j, char* lds) {
  const int tid = threadIdx.x, lane = tid & 63, w = __builtin_amdgcn_readfirstlane(tid >> 6);
  const int g = lane >> 4, l15 = lane & 15, q4 = l15 >> 2, pp = lane & 3;
  const int h = 4 * (x8 & 3) + (j & 3), tile = 8 * (x8 >> 2) + (j >> 2);
  const int rot = (2 * (h & 3) + (tile & 7)) % 7;
  char* ws = p.ws;
  STAMP_DECL
  STAMP(0);
  __builtin_amdgcn_s_setprio(3);
  const u32x4 rX = mkrs4(p.in[0], S * D * 2), rW = mkrs4(p.in[1], D * 3 * D * 2);
  const rsrc_t rWS = mkrs(ws, WS_END);
  const u32 lb = ldsaddr(lds);
  const u32 gen = sload(ws + WS_GEN);
  const int tok0 = tile * 128 + w * 16;
  u32 xvo[2];
#pragma unroll
  for (int i = 0; i < 2; ++i) { int row = 8 * i + (lane >> 3); xvo[i] = (tok0 + row) * (D * 2) + (((lane & 7) ^ ((row >> 1) & 7)) * 16); }
  u32 wvo;
  { int row = 16 * (w & 3) + (lane >> 2); int cs = (lane & 3) ^ (2 * ((row >> 3) & 1)); wvo = row * WROW + h * (DH * 2) + (cs < 3 ? cs * 16 : 40); }
  auto cidx = [&](int i) { int c = rot + i; return c >= 7 ? c - 7 : c; };
  auto issueX = [&](int i) {
    const u32 so = cidx(i) * 128, dst = lb + A_X + (i & 1) * 16384 + w * 2048;
    adma16(rX, dst, xvo[0], so); adma16(rX, dst + 1024, xvo[1], so);
  };
  auto issueW = [&](int v) {
    const u32 dst = lb + A_W + (v % 5) * 8192 + w * 1024;
    if (v < 7) adma16(rW, dst, wvo, cidx(v) * WCH + (1 + (w >> 2)) * (D * 2));
    else adma16(rW, dst, wvo, cidx(2 * (v - 7) + (w >> 2)) * WCH);
  };
  if (w < 2) adma16(mkrs4(p.in[2], H * DH * DH * 2), lb + A_WPHI + w * 1024, lane * 16, h * (DH * DH * 2) + w * 1024);
  else if (w == 2) { int c = rot + (lane >> 3); c = c >= 7 ? c - 7 : c; adma16(mkrs4(p.in[13], D * 2), lb + A_GW, lane < 56 ? c * 128 + (lane & 7) * 16 : 4096, 0); }
  issueW(0); issueX(0);
  __builtin_amdgcn_s_setprio(2);
  issueW(1); issueX(1);
  __builtin_amdgcn_s_setprio(1);
  issueW(2);
  __builtin_amdgcn_s_setprio(APRIO);
  __builtin_amdgcn_sched_barrier(0);
  STAMP(1);
  f32x4 ak[2] = {{0, 0, 0, 0}, {0, 0, 0, 0}}, av[2] = {{0, 0, 0, 0}, {0, 0, 0, 0}};
  u32 xoff[2];
#pragma unroll
  for (int ks = 0; ks < 2; ++ks) xoff[ks] = w * 2048 + l15 * 128 + (((4 * ks + g) ^ ((l15 >> 1) & 7)) * 16);
  u32 woff[2];
#pragma unroll
  for (int ct = 0; ct < 2; ++ct) woff[ct] = (8 * g + q4) * 64 + ((ct ^ (g & 1)) * 32) + pp * 8;
  const u32 goff = w * 2048 + h * 128 + (((lane >> 3) ^ ((h >> 1) & 7)) * 16) + (lane & 7) * 2;
  float gs = 0.f;
  u32x4 xf[14];
  u32x4 wf0[2][2], wf1[2][2];
  u32 gx, gw;
  auto readX = [&](int i) {
    const char* xs = lds + A_X + (i & 1) * 16384;
    xf[2 * i] = *(const u32x4*)(xs + xoff[0]); xf[2 * i + 1] = *(const u32x4*)(xs + xoff[1]);
    gx = *(const u16*)(xs + goff); gw = *(const u16*)(lds + A_GW + i * 128 + lane * 2);
  };
  auto readW = [&](int i, int ks, u32x4 (&wf)[2][2]) {
    const char* wsb = lds + A_W + (i % 5) * 8192;
#pragma unroll
    for (int r = 0; r < 2; ++r)
#pragma unroll
      for (int ct = 0; ct < 2; ++ct) {
        u32x2 a = trread(wsb, woff[ct] + r * 4096 + ks * 2048);
        u32x2 b = trread(wsb, woff[ct] + r * 4096 + ks * 2048 + 256);
        wf[r][ct] = (u32x4){a[0], a[1], b[0], b[1]};
      }
  };
  auto mm = [&](const u32x4& x, const u32x4 (&wf)[2][2]) {
#pragma unroll
    for (int ct = 0; ct < 2; ++ct) { ak[ct] = mfma16(wf[0][ct], x, ak[ct]); av[ct] = mfma16(x, wf[1][ct], av[ct]); }
  };
  waitv<3>();
  BAR();
  CFENCE();
  readX(0); readW(0, 0, wf0);
  __builtin_amdgcn_sched_barrier(0);
  readW(0, 1, wf1);
  __builtin_amdgcn_sched_barrier(0);
  WAIT_L(8);
  issueX(2); issueW(3);
#pragma unroll
  for (int i = 0; i < 7; ++i) {
    __builtin_amdgcn_sched_barrier(0);
    const u32 gx0 = gx, gw0 = gw;
    mm(xf[2 * i], wf0);
    gs += rbf(smul(bflo(gx0), bflo(gw0)));
    asm volatile("" : "+v"(gs));
    __builtin_amdgcn_sched_barrier(0);
    if (i < 6) {
      if (i == 0 || i == 3 || i == 4) waitv<4>(); else if (i == 5) { if (w < 4) waitv<3>(); else waitv<2>(); } else waitv<5>();
      readX(i + 1); readW(i + 1, 0, wf0);
    }
    __builtin_amdgcn_sched_barrier(0);
    mm(xf[2 * i + 1], wf1);
    __builtin_amdgcn_sched_barrier(0);
    if (i == 1 && w == 7) {
      u32x4 wph[2];
#pragma unroll
      for (int ft = 0; ft < 2; ++ft) {
        int f = 7 * q4 + 4 * ft + pp; f = f > 27 ? 27 : f;
        const char* wp = lds + A_WPHI + f * 2;
        const int d0 = 4 * g, d1 = g < 2 ? 16 + 4 * g : 24;
        u32 e[8];
#pragma unroll
        for (int jj = 0; jj < 4; ++jj) { e[jj] = *(const u16*)(wp + (d0 + jj) * 56); e[4 + jj] = *(const u16*)(wp + (d1 + jj) * 56); }
        wph[ft] = (u32x4){e[0] | (e[1] << 16), e[2] | (e[3] << 16), g == 2 ? 0u : (e[4] | (e[5] << 16)), g == 2 ? 0u : (e[6] | (e[7] << 16))};
      }
      CFENCE();
      *(u32x4*)(lds + A_WPHI + lane * 16) = wph[0]; *(u32x4*)(lds + A_WPHI + 1024 + lane * 16) = wph[1];
      WAIT_L(0);
    }
    if (i < 6) {
      readW(i + 1, 1, wf1);
      __builtin_amdgcn_sched_barrier(0);
      WAIT_L(8);
      if (i + 3 < 7) issueX(i + 3);
      if (i == 0) issueW(4);
      if (i < 5) {
        if (i < 3) waitv<6>();
        BAR();
        CFENCE();
        issueW(i + 5);
        if (i == 4 && w < 4) adma16(rW, lb + A_Q6 + w * 1024, wvo, cidx(6) * WCH);
      }
    }
  }
  __builtin_amdgcn_s_setprio(2);
  STAMP(2);
  const u32 tag = gen + 1u, t16 = tag & 0xffffu;
  char* phk = lds + A_X; char* vt = lds + A_VT;
  {
    const int tl = w * 16 + l15;
    u32x4 kb = {pk2(ak[0][0], ak[0][1]), pk2(ak[0][2], ak[0][3]), pk2(ak[1][0], ak[1][1]), pk2(ak[1][2], ak[1][3])};
    f32x4 z4 = {0, 0, 0, 0}; f32x4 pk[2];
    pk[0] = mfma16(*(const u32x4*)(lds + A_WPHI + lane * 16), kb, z4); pk[1] = mfma16(*(const u32x4*)(lds + A_WPHI + 1024 + lane * 16), kb, z4);
    u32x4 pos, neg; dual_softmax(pk, pos, neg);
    const int sw = ((tl >> 1) & 1) | (((tl >> 3) & 1) << 1);
    char* rowp = phk + tl * 128 + g * 8;
    *(u32x2*)(rowp + ((0 ^ sw) * 32)) = (u32x2){pos[0], pos[1]};
    *(u32x2*)(rowp + ((1 ^ sw) * 32)) = (u32x2){pos[2], pos[3]};
    *(u32x2*)(rowp + ((2 ^ sw) * 32)) = (u32x2){neg[0], neg[1]};
    *(u32x2*)(rowp + ((3 ^ sw) * 32)) = (u32x2){neg[2], neg[3]};
  }
  STAMP(3);
  {
#pragma unroll
    for (int ct = 0; ct < 2; ++ct) {
      int d = ct == 0 ? l15 : (l15 < 8 ? 16 + l15 : 12 + l15);
      int G = (d * 37) >> 8, e = d - 7 * G;
      int rho = e < 4 ? 4 * G + e : 12 + 4 * G + e;
      f32x4 v = av[ct];
      if (ct == 1 && (l15 >> 2) == 2) { rho = pp == 0 ? 27 : (pp == 1 ? 19 : (pp == 2 ? 23 : 31)); if (pp == 0) v = (f32x4){1.f, 1.f, 1.f, 1.f}; }
      *(u32x2*)(vt + rho * 256 + (((2 * w + (g >> 1)) ^ (rho & 15)) * 16) + (g & 1) * 8) = (u32x2){pk2(v[0], v[1]), pk2(v[2], v[3])};
    }
  }
  bar_l();
  STAMP(4);
  if (w < 4) {
    const int sg = w >> 1, dt = w & 1;
    f32x4 t0 = {0, 0, 0, 0}, t1 = t0;
#pragma unroll
    for (int ks = 0; ks < 4; ++ks) {
      u32x4 af[2];
#pragma unroll
      for (int ft = 0; ft < 2; ++ft) {
        u32x2 a[2];
#pragma unroll
        for (int t = 0; t < 2; ++t) {
          int row = 32 * ks + 8 * g + 4 * t + q4;
          int sw = ((row >> 1) & 1) | (((row >> 3) & 1) << 1);
          a[t] = trread(phk, row * 128 + (((2 * sg + ft) ^ sw) * 32) + pp * 8);
        }
        af[ft] = (u32x4){a[0][0], a[0][1], a[1][0], a[1][1]};
      }
      u32x4 bf = *(const u32x4*)(vt + (16 * dt + l15) * 256 + (((4 * ks + g) ^ l15) * 16));
      t0 = mfma16(af[0], bf, t0); t1 = mfma16(af[1], bf, t1);
    }
    u32x4 un = {pk2(t0[0], t0[1]), pk2(t0[2], t0[3]), pk2(t1[0], t1[1]), (pk2(t1[2], t1[2]) & 0xffffu) | (t16 << 16)};
    st16c(un, rWS, (((h * 16 + tile) * 4 + w) * 64 + lane) * 16, WS_KVP);
    waitv<1>();
  } else waitv<0>();
  STAMP(5);
  BAR();
  CFENCE();
  if (tid == 511) __hip_atomic_store((u32*)(ws + flag_off(F_A + blockIdx.x)), tag, __ATOMIC_RELAXED, __HIP_MEMORY_SCOPE_AGENT);
  u32x4 pqp, pqn;
  u32x4 pv[16];
  const u32 pvo = ((h * 16 * 4 + (w & 3)) * 64 + lane) * 16;
  {
    f32x4 aq[2] = {{0, 0, 0, 0}, {0, 0, 0, 0}};
#pragma unroll
    for (int i = 0; i < 7; ++i) {
      const char* qb = lds + (i == 6 ? A_Q6 : A_W + ((7 + (i >> 1)) % 5) * 8192 + (i & 1) * 4096);
#pragma unroll
      for (int ks = 0; ks < 2; ++ks)
#pragma unroll
        for (int ct = 0; ct < 2; ++ct) {
          u32x2 a = trread(qb, woff[ct] + ks * 2048);
          u32x2 b = trread(qb, woff[ct] + ks * 2048 + 256);
          aq[ct] = mfma16((u32x4){a[0], a[1], b[0], b[1]}, xf[2 * i + ks], aq[ct]);
        }
    }
    const u32x4 wq0 = *(const u32x4*)(lds + A_WPHI + lane * 16), wq1 = *(const u32x4*)(lds + A_WPHI + 1024 + lane * 16);
    bar_l();
    if (w < 4) {
#pragma unroll
      for (int t = 0; t < 16; ++t) pv[t] = ld16c(rWS, pvo, WS_KVP + t * 4096);
    }
    __builtin_amdgcn_sched_barrier(0);
    u32x4 qb = {pk2(aq[0][0], aq[0][1]), pk2(aq[0][2], aq[0][3]), pk2(aq[1][0], aq[1][1]), pk2(aq[1][2], aq[1][3])};
    f32x4 z4 = {0, 0, 0, 0}; f32x4 pq[2];
    pq[0] = mfma16(wq0, qb, z4); pq[1] = mfma16(wq1, qb, z4);
    dual_softmax(pq, pqp, pqn);
    __builtin_amdgcn_sched_barrier(0);
    const bool fin0 = j < 28;
    if (w >= 4 && fin0) {
      const u32x4 rWO = mkrs4(p.in[3], D * D * 2);
      const int jr = lane >> 3;
      const u32 vo = (jr < 7 ? jr : 6) * (D * 2) + (j % 7) * 128 + (((lane & 7) ^ (2 * (((jr >> 1) & 1) | ((w & 1) << 1)))) * 16);
#pragma unroll
      for (int m = 0; m < 16; ++m) adma16(rWO, lb + ((w - 4) + 4 * m) * 1024, vo, ((w - 4) + 4 * m) * (7 * D * 2));
    }
  }
  STAMP(6);
  const bool fin = j < 28;
  const int tt = 4 * x8 + j / 7, cg = j % 7, c0 = cg * 64;
  const int rt = w >> 1, kh = w & 1;
  const int ftok = tt * 64 + rt * 16 + l15;
  if (w < 4) {
    u32 pend = 15;
#pragma unroll 1
    for (int it = 0; it < MAXSPIN; ++it) {
#pragma unroll
      for (int gq = 0; gq < 4; ++gq)
        if (pend & (1u << gq)) {
          const u32 m = (pv[4 * gq][3] ^ (t16 << 16)) | (pv[4 * gq + 1][3] ^ (t16 << 16)) | (pv[4 * gq + 2][3] ^ (t16 << 16)) | (pv[4 * gq + 3][3] ^ (t16 << 16));
          if (__all(m < 0x10000u)) pend &= ~(1u << gq);
        }
      if (!pend) break;
      __builtin_amdgcn_s_sleep(1);
#pragma unroll
      for (int gq = 0; gq < 4; ++gq)
        if (pend & (1u << gq)) {
#pragma unroll
          for (int t = 0; t < 4; ++t) pv[4 * gq + t] = ld16c(rWS, pvo, WS_KVP + (4 * gq + t) * 4096);
        }
    }
    float s[7] = {0, 0, 0, 0, 0, 0, 0};
    u32 selo = 0x00003f80u, sehi = 0x3f800000u;
    asm volatile("" : "+s"(selo), "+s"(sehi));
#pragma unroll
    for (int t = 0; t < 16; ++t) {
      s[0] = acc_sel(pv[t][0], selo, s[0]); s[1] = acc_sel(pv[t][0], sehi, s[1]); s[2] = acc_sel(pv[t][1], selo, s[2]); s[3] = acc_sel(pv[t][1], sehi, s[3]);
      s[4] = acc_sel(pv[t][2], selo, s[4]); s[5] = acc_sel(pv[t][2], sehi, s[5]); s[6] += bflo(pv[t][3]);
    }
    *(u32x4*)(lds + A_KVB + w * 1024 + lane * 16) = (u32x4){pk2(s[0], s[1]), pk2(s[2], s[3]), pk2(s[4], s[5]), pk2(s[6], 0.f)};
  }
  const float gate = rbf(__builtin_amdgcn_rcpf(1.f + __expf(-rbf(red_sum_all(gs)))));
  bar_l();
  STAMP(7);
  {
    f32x4 o[2];
#pragma unroll
    for (int dt = 0; dt < 2; ++dt) {
      const u32x4 kp = *(const u32x4*)(lds + A_KVB + dt * 1024 + lane * 16), kn = *(const u32x4*)(lds + A_KVB + (2 + dt) * 1024 + lane * 16);
      o[dt] = mfma16(kp, pqp, (f32x4){0, 0, 0, 0});
      o[dt] = mfma16(kn, pqn, o[dt]);
    }
    u32 du = __float_as_uint(o[1][3]);
    auto r1 = __builtin_amdgcn_permlane32_swap(du, du, false, false);
    u32 d1 = r1[1];
    auto r2 = __builtin_amdgcn_permlane16_swap(d1, d1, false, false);
    float den = __uint_as_float(r2[0]);
    den = rbf(rbf(den) + 1e-6f);
    const float rd = __builtin_amdgcn_rcpf(den);
    const u32 n0 = pk2(o[0][0], o[0][1]), n1 = pk2(o[0][2], o[0][3]), n2 = pk2(o[1][0], o[1][1]), n3 = pk2(o[1][2], o[1][2]);
    u32x4 of = {pk2(bflo(n0) * rd, bfhi(n0) * rd), pk2(bflo(n1) * rd, bfhi(n1) * rd), pk2(bflo(n2) * rd, bfhi(n2) * rd), (pk2(bflo(n3) * rd, 0.f) & 0xffffu) | (t16 << 16)};
    const int rt16 = tile * 8 + w;
    st16c(of, rWS, ((rt16 * 16 + h) * 64 + lane) * 16, WS_OF);
    if (lane == 0) st4c((__float_as_uint(gate) >> 16) | (t16 << 16), rWS, (tok0 + h) * 4, WS_GATE);
  }
  STAMP(8);
  if (fin) {
    const int rt16 = tt * 4 + rt;
    u32x2 xin[2], dsk[2];
#pragma unroll
    for (int m = 0; m < 2; ++m) {
      xin[m] = *(const u32x2*)(p.in[0] + (size_t)ftok * D + c0 + 32 * kh + 16 * m + 4 * g);
      dsk[m] = *(const u32x2*)(p.in[12] + c0 + 32 * kh + 16 * m + 4 * g);
    }
    u32x4 of[8];
    u32 yv[2][4], gv = 0;
    const u32 ovo = ((rt16 * 16 + 8 * kh) * 64 + lane) * 16;
    const u32* yflag = (const u32*)(ws + flag_off(F_Y + (c0 + 32 * kh + (lane & 31)) * 4 + (tt >> 3)));
    u32 pend = 15;
    u32 fl = __hip_atomic_load(yflag, __ATOMIC_RELAXED, __HIP_MEMORY_SCOPE_AGENT);
#pragma unroll
    for (int t = 0; t < 8; ++t) of[t] = ld16c(rWS, ovo, WS_OF + t * 1024);
    gv = ld4c(rWS, ftok * 4, WS_GATE);
#pragma unroll
    for (int m = 0; m < 2; ++m)
#pragma unroll
      for (int i = 0; i < 4; ++i) yv[m][i] = 0;
#pragma unroll 1
    for (int it = 0; it < MAXSPIN; ++it) {
      if ((pend & 8) && __all(fl == tag)) {
        pend &= ~8u;
        CFENCE();
#pragma unroll
        for (int m = 0; m < 2; ++m)
#pragma unroll
          for (int i = 0; i < 4; ++i) yv[m][i] = ld2c(rWS, (c0 + 32 * kh + 4 * g) * (S * 2) + ftok * 2, WS_YT + (16 * m + i) * (S * 2));
      }
#pragma unroll
      for (int gq = 0; gq < 2; ++gq)
        if (pend & (1u << gq)) {
          const u32 m = (of[4 * gq][3] ^ (t16 << 16)) | (of[4 * gq + 1][3] ^ (t16 << 16)) | (of[4 * gq + 2][3] ^ (t16 << 16)) | (of[4 * gq + 3][3] ^ (t16 << 16));
          if (__all(m < 0x10000u)) pend &= ~(1u << gq);
        }
      if ((pend & 4) && __all((gv >> 16) == t16)) pend &= ~4u;
      if (!pend) break;
      __builtin_amdgcn_s_sleep(1);
      if (pend & 8) fl = __hip_atomic_load(yflag, __ATOMIC_RELAXED, __HIP_MEMORY_SCOPE_AGENT);
#pragma unroll
      for (int gq = 0; gq < 2; ++gq)
        if (pend & (1u << gq)) {
#pragma unroll
          for (int t = 0; t < 4; ++t) of[4 * gq + t] = ld16c(rWS, ovo, WS_OF + (4 * gq + t) * 1024);
        }
      if (pend & 4) gv = ld4c(rWS, ftok * 4, WS_GATE);
    }
    STAMP(9);
    waitv<0>();
    BAR();
    CFENCE();
    u32 wa[4];
    { const int s = 2 * (((q4 >> 1) & 1) | ((g & 1) << 1));
#pragma unroll
      for (int ct = 0; ct < 4; ++ct) wa[ct] = kh * 32768 + (8 * g + q4) * 128 + (((2 * ct) ^ s) * 16) + pp * 8; }
    f32x4 acc[4];
#pragma unroll
    for (int ct = 0; ct < 4; ++ct) acc[ct] = (f32x4){0, 0, 0, 0};
#pragma unroll
    for (int hh = 0; hh < 8; ++hh) {
      u32x4 ob = of[hh]; ob[3] &= 0xffffu;
#pragma unroll
      for (int ct = 0; ct < 4; ++ct) {
        u32x2 a = trread(lds, wa[ct] + hh * 4096);
        u32x2 b = trread(lds, wa[ct] + hh * 4096 + 512);
        acc[ct] = mfma16((u32x4){a[0], a[1], b[0], b[1]}, ob, acc[ct]);
      }
    }
    STAMP(10);
    const f32x4 c0_ = acc[0], c1_ = acc[1], c2_ = acc[2], c3_ = acc[3];
    {
      f32x4* rw = (f32x4*)(lds + A_RED + w * 2048) + lane;
      rw[0] = kh ? c0_ : c2_; rw[64] = kh ? c1_ : c3_;
    }
    const f32x4 keep0 = kh ? c2_ : c0_, keep1 = kh ? c3_ : c1_;
    bar_l();
    const f32x4* rr = (const f32x4*)(lds + A_RED + (w ^ 1) * 2048) + lane;
    const float gg = bflo(gv), og = rbf(1.f - gg);
    u32 res[2][2];
#pragma unroll
    for (int m = 0; m < 2; ++m) {
      f32x4 a = rr[m * 64];
      a += m ? keep1 : keep0;
      const float xx[4] = {bflo(xin[m][0]), bfhi(xin[m][0]), bflo(xin[m][1]), bfhi(xin[m][1])};
      const float ds[4] = {bflo(dsk[m][0]), bfhi(dsk[m][0]), bflo(dsk[m][1]), bfhi(dsk[m][1])};
      float r[4];
#pragma unroll
      for (int i = 0; i < 4; ++i) {
        float av_ = rbf(a[i]);
        float b = rbf(bflo(yv[m][i]) + rbf(smul(xx[i], ds[i])));
        r[i] = rbf(smul(gg, av_)) + rbf(smul(og, b));
      }
      res[m][0] = pk2(r[0], r[1]); res[m][1] = pk2(r[2], r[3]);
    }
    u32x4 ov;
    {
      auto s0 = __builtin_amdgcn_permlane32_swap(res[0][0], res[1][0], false, false);
      auto s1 = __builtin_amdgcn_permlane32_swap(res[0][1], res[1][1], false, false);
      auto t0 = __builtin_amdgcn_permlane16_swap(s0[0], s0[1], false, false);
      auto t1 = __builtin_amdgcn_permlane16_swap(s1[0], s1[1], false, false);
      ov = (u32x4){t0[0], t1[0], t0[1], t1[1]};
    }
    *(u32x4*)(p.out + (size_t)ftok * D + c0 + 32 * kh + 8 * g) = ov;
    STAMP(11);
  }
  else if (x8 == 0 && j == 31) {
    pollf(ws, tid < 224 ? F_XT + tid * 8 : (tid < 480 ? F_A + (tid - 224) : F_A), tag);
    BAR();
    if (tid == 0) __hip_atomic_store((u32*)(ws + WS_GEN), tag, __ATOMIC_RELAXED, __HIP_MEMORY_SCOPE_AGENT);
  }
  STAMP_FLUSH(ws);
}

DI f32x2 cmul(f32x2 a, f32x2 b) {
  f32x2 m, r;
  asm("v_pk_mul_f32 %0, %1, %2 op_sel:[1,0] op_sel_hi:[1,1]" : "=v"(m) : "v"(b), "v"(a));
  asm("v_pk_fma_f32 %0, %1, %2, %3 op_sel:[0,0,1] op_sel_hi:[1,0,0] neg_lo:[0,0,1]" : "=v"(r) : "v"(a), "v"(b), "v"(m));
  return r;
}
DI f32x2 cexp_pow(float mr, float pr, float a) {
  float m = __builtin_amdgcn_exp2f(mr * a);
  float t = __builtin_amdgcn_fractf(pr * a);
  return (f32x2){m * __builtin_amdgcn_cosf(t), m * __builtin_amdgcn_sinf(t)};
}
constexpr int KG_ROW = 272, KG_SLAB = 16 * KG_ROW;
constexpr int B_UC = 34816, UCH = 16656, B_KR = 68128, B_KTAB = 77344, B_DUM = 80416, B_TIMG = B_KR, B_RED = 0;
constexpr int UC0 = 8, UC1 = 4176, UC2 = 8336, UC3 = 12496;

DI void roleB(const Params& p, int x8, int jb, char* lds) {
  const int tid = threadIdx.x, lane = tid & 63, w = __builtin_amdgcn_readfirstlane(tid >> 6);
  const int g = lane >> 4, l15 = lane & 15;
  char* ws = p.ws;
  STAMP_DECL
  STAMP(0);
  __builtin_amdgcn_s_setprio(3);
  const rsrc_t rWS = mkrs(ws, WS_END);
  const int tt = 4 * x8 + jb / 7, cg = jb % 7, ch0 = 2 * (x8 * 28 + jb);
  const u32 gen = sload(ws + WS_GEN);
  if (w == 7) {
    const u32x4 rc = mkrs4((const void*)(__builtin_amdgcn_s_getpc() & ~(unsigned long long)127), CODE_BYTES);
#pragma unroll
    for (int i = 0; i < (CODE_BYTES + 8191) / 8192; ++i) atouch4(rc, ldsaddr(lds) + B_DUM, lane * 128, i * 8192);
  } else if (w >= 5) {
    atouch4(mkrs4(p.in[3], D * D * 2), ldsaddr(lds) + B_DUM, lane * 128, (jb * 2 + (w - 5)) * 7168);
  }
  const u32x4 xv = *(const u32x4*)(p.in[0] + (size_t)(tt * 64 + (tid >> 3)) * D + cg * 64 + (tid & 7) * 8);
  const int d = ch0 + (w >> 2);
  const int mo = d * NM + (w & 3) * 64 + lane;
  const u32 are = p.in[5][mo], aim = p.in[6][mo], bre = p.in[7][mo], bim = p.in[8][mo], cre = p.in[9][mo], cim = p.in[10][mo];
  const u32 ldt = p.in[11][d];
  __builtin_amdgcn_sched_barrier(0);
  {
    char* img = lds + B_TIMG;
    u32* dst = (u32*)(img + (tid >> 3) * 132 + (tid & 7) * 16);
    dst[0] = xv[0]; dst[1] = xv[1]; dst[2] = xv[2]; dst[3] = xv[3];
    bar_l();
    const int chl = tid >> 3, oc = tid & 7;
    u32 o[4];
#pragma unroll
    for (int jj = 0; jj < 4; ++jj) {
      u32 lo = *(const u16*)(img + (8 * oc + 2 * jj) * 132 + chl * 2);
      u32 hi = *(const u16*)(img + (8 * oc + 2 * jj + 1) * 132 + chl * 2);
      o[jj] = lo | (hi << 16);
    }
    st16c((u32x4){o[0], o[1], o[2], o[3]}, rWS, (cg * 64 + chl) * (S * 2) + tt * 128 + oc * 16, WS_XT);
  }
  const u32 tag = gen + 1u;
  __builtin_amdgcn_s_setprio(BPRIO);
  STAMP(1);
  char* slab = lds + w * KG_SLAB;
  const int cch = tid >> 8, cq = tid & 255;
  u32x4 ua; u32x2 ub;
  f32x4 acc[3];
  {
    const float dt = rbf(__expf(bflo(ldt)));
    const float ar = dt * bflo(are), ai = dt * bflo(aim), br = dt * bflo(bre), bi = dt * bflo(bim), cr = bflo(cre), ci = bflo(cim);
    const float mr = ar * 1.4426950408889634f, pr = ai * 0.15915494309189535f;
    f32x2 W = {cr * br - ci * bi, -(cr * bi + ci * br)};
    f32x2 z1c = cexp_pow(mr, -pr, 1.f);
    f32x2 z8 = cexp_pow(mr, pr, 8.f);
    f32x2 a128 = cexp_pow(mr, -pr, 128.f);
    asm volatile("s_mov_b32 m0, %0" ::"s"(ldsaddr(slab)) : "memory");
#define WROWT(r, v) do { const f32x2 v_ = (v); asm volatile("ds_write_addtid_b32 %0 offset:%1" ::"v"(pk2(v_.x, v_.y)), "i"((r) * KG_ROW) : "memory"); } while (0)
    const char* rd = slab + l15 * KG_ROW + g * 16;
    u32x4 yf[4];
    {
      f32x2 y[16];
      y[1] = z8;
      f32x2 y1c = z8; asm volatile("" : "+v"(y1c));
      y[2] = cmul(y[1], y1c); y[3] = cmul(y[2], y[1]); y[4] = cmul(y[3], y[1]);
      y[5] = cmul(y[4], y[1]); y[6] = cmul(y[4], y[2]); y[7] = cmul(y[4], y[3]); y[8] = cmul(y[5], y[3]);
#pragma unroll
      for (int c = 9; c < 16; ++c) y[c] = cmul(y[8], y[c - 8]);
      WROWT(0, ((f32x2){1.f, 0.f}));
#pragma unroll
      for (int c = 1; c < 16; ++c) WROWT(c, y[c]);
    }
#pragma unroll
    for (int ks = 0; ks < 4; ++ks) yf[ks] = *(const u32x4*)(rd + ks * 64);
    CFENCE();
    f32x2 R0[8];
    {
      f32x2 zc = z1c; asm volatile("" : "+v"(zc));
      f32x2 z2 = cmul(z1c, zc);
      f32x2 z2c = z2; asm volatile("" : "+v"(z2c));
      f32x2 z4 = cmul(z2, z2c);
      R0[0] = W; R0[1] = cmul(W, z1c); R0[2] = cmul(W, z2); R0[3] = cmul(R0[1], z2);
#pragma unroll
      for (int b = 0; b < 4; ++b) R0[4 + b] = cmul(R0[b], z4);
    }
    f32x2 A[17];
    {
      A[1] = a128;
      f32x2 a1c = a128; asm volatile("" : "+v"(a1c));
      A[2] = cmul(A[1], a1c); A[3] = cmul(A[2], A[1]); A[4] = cmul(A[3], A[1]);
      A[5] = cmul(A[4], A[1]); A[6] = cmul(A[4], A[2]); A[7] = cmul(A[4], A[3]); A[8] = cmul(A[5], A[3]);
#pragma unroll
      for (int m = 9; m < 16; ++m) A[m] = cmul(A[8], A[m - 8]);
      A[16] = cmul(A[9], A[7]);
    }
    {
      f32x2 t[8];
#pragma unroll
      for (int b = 0; b < 8; ++b) t[b] = cmul(R0[b], A[1]);
#pragma unroll
      for (int b = 0; b < 8; ++b) { WROWT(b, R0[b]); WROWT(8 + b, t[b]); }
    }
    acc[0] = (f32x4){0, 0, 0, 0};
#pragma unroll
    for (int ks = 0; ks < 4; ++ks) { u32x4 xf = *(const u32x4*)(rd + ks * 64); acc[0] = mfma16(xf, yf[ks], acc[0]); }
    CFENCE();
    STAMP(7);
    publish(ws, F_XT + (tt * 7 + cg) * 8 + w, tag, lane);
    STAMP(8);
    {
      f32x2 t[16];
#pragma unroll
      for (int hh = 0; hh < 2; ++hh)
#pragma unroll
        for (int b = 0; b < 4; ++b) t[b + 4 * hh] = cmul(R0[2 * b], A[2 + hh]);
#pragma unroll
      for (int hh = 0; hh < 4; ++hh)
#pragma unroll
        for (int b = 0; b < 2; ++b) t[8 + b + 2 * hh] = cmul(R0[4 * b], A[4 + hh]);
#pragma unroll
      for (int r = 0; r < 16; ++r) WROWT(r, t[r]);
    }
    acc[1] = (f32x4){0, 0, 0, 0};
#pragma unroll
    for (int ks = 0; ks < 4; ++ks) { u32x4 xf = *(const u32x4*)(rd + ks * 64); acc[1] = mfma16(xf, yf[ks], acc[1]); }
    CFENCE();
    STAMP(9);
    {
      const int chx = ch0 + cch;
      int t9 = 8 * (w & 3) + (lane < 8 ? lane : 8); t9 = t9 > 31 ? 31 : t9;
      pollf(ws, F_XT + (t9 * 7 + (chx >> 6)) * 8 + ((chx & 63) >> 3), tag);
      STAMP(10);
      ua = ld16c(rWS, chx * (S * 2) + cq * 16, WS_XT);
      ub = ld8c(rWS, chx * (S * 2) + (cq < 255 ? cq + 1 : cq) * 16, WS_XT);
    }
    {
      f32x2 t[9];
#pragma unroll
      for (int hh = 0; hh < 9; ++hh) t[hh] = cmul(W, A[8 + hh]);
#pragma unroll
      for (int hh = 0; hh < 9; ++hh) WROWT(hh, t[hh]);
    }
    acc[2] = (f32x4){0, 0, 0, 0};
#pragma unroll
    for (int ks = 0; ks < 4; ++ks) { u32x4 xf = *(const u32x4*)(rd + ks * 64); acc[2] = mfma16(xf, yf[ks], acc[2]); }
    CFENCE();
    float* kt = (float*)slab;
    *(f32x4*)(kt + 8 * l15 + 4 * (g & 1) + 128 * (g >> 1)) = acc[0];
    {
      const int t0 = g < 2 ? 256 + 4 * l15 + 64 * g : 384 + 2 * l15 + 64 * (g - 2);
      const int t1 = g < 2 ? t0 + 2 : t0 + 32;
      *(f32x2*)(kt + t0) = (f32x2){acc[1][0], acc[1][1]};
      *(f32x2*)(kt + t1) = (f32x2){acc[1][2], acc[1][3]};
    }
#pragma unroll
    for (int i = 0; i < 4; ++i) kt[512 + l15 + 16 * (4 * g + i)] = acc[2][i];
  }
  STAMP(2);
  {
    if (cq == 255) { ub[0] = 0; ub[1] = 0; }
    const u32 e[6] = {ua[0], ua[1], ua[2], ua[3], ub[0], ub[1]};
    char* base = lds + B_UC + cch * UCH + (16 + cq * 8) * 2;
    u32 sh[5];
#pragma unroll
    for (int q = 0; q < 5; ++q) sh[q] = __builtin_amdgcn_alignbit(e[q + 1], e[q], 16);
    *(u32x2*)(base + UC0) = (u32x2){e[0], e[1]}; *(u32x2*)(base + UC0 + 8) = (u32x2){e[2], e[3]};
    *(u32x4*)(base + UC1) = (u32x4){sh[0], sh[1], sh[2], sh[3]};
    *(u32x4*)(base + UC2) = (u32x4){e[1], e[2], e[3], e[4]};
    *(u32x4*)(base + UC3) = (u32x4){sh[1], sh[2], sh[3], sh[4]};
    if (cq == 0) {
      char* b0 = lds + B_UC + cch * UCH;
      const u32x4 z = {0, 0, 0, 0};
      *(u32x2*)(b0 + UC0) = (u32x2){0, 0}; *(u32x2*)(b0 + UC0 + 8) = (u32x2){0, 0}; *(u32x2*)(b0 + UC0 + 16) = (u32x2){0, 0}; *(u32x2*)(b0 + UC0 + 24) = (u32x2){0, 0};
      *(u32x4*)(b0 + UC1) = z; *(u32x4*)(b0 + UC1 + 16) = (u32x4){0, 0, 0, e[0] << 16};
      *(u32x4*)(b0 + UC2) = z; *(u32x4*)(b0 + UC2 + 16) = (u32x4){0, 0, 0, e[0]};
      *(u32x4*)(b0 + UC3) = z; *(u32x4*)(b0 + UC3 + 16) = (u32x4){0, 0, e[0] << 16, sh[0]};
    }
  }
  __builtin_amdgcn_s_setprio(0);
  bar_l();
  STAMP(3);
  if (tid < 384) {
    const int ch = tid >= 192 ? 1 : 0, t4 = (tid - 192 * ch) * 4;
    const float* k0 = (const float*)(lds + 4 * ch * KG_SLAB) + t4;
    f32x4 s = *(const f32x4*)k0;
#pragma unroll
    for (int q = 1; q < 4; ++q) s += *(const f32x4*)(k0 + q * (KG_SLAB / 4));
    *(u32x2*)(lds + B_KTAB + ch * 1536 + t4 * 2) = (u32x2){pk2(s[0], s[1]), pk2(s[2], s[3])};
  }
  bar_l();
  {
    const int jhi = 16 * (cq >> 1) + 15 - (cq & 1) * 8;
    const float fj = (float)jhi;
    const char* kt = lds + B_KTAB + cch * 1536;
    u32 o[4];
#pragma unroll
    for (int e = 0; e < 4; ++e) {
      u32 bits = pk2(fj - (float)(2 * e), fj - (float)(2 * e + 1));
      u32 t0 = (bits & 0xffffu) - 0x4280u, t1 = (bits >> 16) - 0x4280u;
      if (jhi < 256) { t0 = jhi - 2 * e; t1 = jhi - 2 * e - 1; }
      u32 lo = *(const u16*)(kt + t0 * 2), hi = *(const u16*)(kt + t1 * 2);
      o[e] = lo | (hi << 16);
    }
    char* kr = lds + B_KR + cch * 4608;
    *(u32x4*)(kr + 512 + cq * 16) = (u32x4){o[0], o[1], o[2], o[3]};
    if (cq < 32) *(u32x4*)(kr + cq * 16) = (u32x4){0, 0, 0, 0};
  }
  bar_l();
  STAMP(4);
  {
    const int ch = w >> 2, rh = (w >> 1) & 1, sh = w & 1;
    const int idx0 = 8 * g + l15 + 1, cc = idx0 & 3;
    const u32 ucb = cc == 0 ? UC0 : (cc == 1 ? UC1 : (cc == 2 ? UC2 : UC3));
    const LAS char* abase = (const LAS char*)(lds + B_UC + ch * UCH + ucb + (idx0 & ~3) * 2 + rh * 256);
    const char* bbase = lds + B_KR + ch * 4608 + (256 + 16 * (l15 - (g >> 1)) + 8 * (g & 1)) * 2 - rh * 256;
    f32x4 c4[4];
#pragma unroll
    for (int i = 0; i < 4; ++i) c4[i] = (f32x4){0, 0, 0, 0};
    if (sh == 0) {
#pragma unroll 1
      for (int rr = 0; rr < 4; ++rr) {
        u32x4 af[8], bf[8];
#pragma unroll
        for (int a = 0; a < 8; ++a) {
          const LAS char* pa = abase + (256 * a) * 2 + rr * 64;
          u32x2 lo = *(const volatile LAS u32x2*)pa, hi = *(const volatile LAS u32x2*)(pa + 8);
          af[a] = (u32x4){lo[0], lo[1], hi[0], hi[1]};
          bf[a] = *(const u32x4*)(bbase + (256 * a) * 2 - rr * 64);
        }
        c4[0] = mfma16(af[0], bf[0], c4[0]);
        c4[1] = mfma16(af[0], bf[1], c4[1]); c4[1] = mfma16(af[1], bf[0], c4[1]);
#pragma unroll
        for (int a = 0; a < 8; ++a) {
          if (a < 7) c4[2] = mfma16(af[a], bf[6 - a], c4[2]);
          c4[3] = mfma16(af[a], bf[7 - a], c4[3]);
        }
      }
    } else {
#pragma unroll 1
      for (int rr = 0; rr < 4; ++rr) {
        u32x4 af[6], bf[6];
#pragma unroll
        for (int a = 0; a < 6; ++a) {
          const LAS char* pa = abase + (256 * a) * 2 + rr * 64;
          u32x2 lo = *(const volatile LAS u32x2*)pa, hi = *(const volatile LAS u32x2*)(pa + 8);
          af[a] = (u32x4){lo[0], lo[1], hi[0], hi[1]};
          bf[a] = *(const u32x4*)(bbase + (256 * a) * 2 - rr * 64);
        }
#pragma unroll
        for (int a = 0; a < 6; ++a) {
          if (a < 3) c4[0] = mfma16(af[a], bf[2 - a], c4[0]);
          if (a < 4) c4[1] = mfma16(af[a], bf[3 - a], c4[1]);
          if (a < 5) c4[2] = mfma16(af[a], bf[4 - a], c4[2]);
          c4[3] = mfma16(af[a], bf[5 - a], c4[3]);
        }
      }
    }
    STAMP(5);
    const f32x4 e0 = c4[0], e1 = c4[1], e2 = c4[2], e3 = c4[3];
    f32x4* xw = (f32x4*)(lds + B_RED + w * 2048) + lane;
    xw[0] = rh ? e0 : e2; xw[64] = rh ? e1 : e3;
    bar_l();
    const f32x4* xr = (const f32x4*)(lds + B_RED + (w ^ 2) * 2048) + lane;
    f32x4 v0 = xr[0], v1 = xr[64];
    v0 += rh ? e2 : e0; v1 += rh ? e3 : e1;
    const int pt = sh == 0 ? (rh ? 6 : 0) : (rh ? 4 : 2);
    auto s0 = __builtin_amdgcn_permlane16_swap(pk2(v0[0], v0[1]), pk2(v1[0], v1[1]), false, false);
    auto s1 = __builtin_amdgcn_permlane16_swap(pk2(v0[2], v0[3]), pk2(v1[2], v1[3]), false, false);
    st16c((u32x4){s0[0], s1[0], s0[1], s1[1]}, rWS, (ch0 + ch) * (S * 2) + (256 * (pt + (g & 1)) + 16 * l15 + 8 * (g >> 1)) * 2, WS_YT);
    publish(ws, F_Y + (ch0 + ch) * 4 + (pt >> 1), tag, lane);
    STAMP(6);
  }
  STAMP_FLUSH(ws);
}

__global__ __launch_bounds__(512, 4) void fused(Params p) {
  __shared__ __attribute__((aligned(16))) char lds[LDS_BYTES];
  const int b = blockIdx.x, x8 = b & 7, j = b >> 3;
  if (j < 32) roleA(p, x8, j, lds);
  else roleB(p, x8, j - 32, lds);
}

extern "C" void kernel_launch(void* const* d_in, const int* in_sizes, int n_in, void* d_out, int out_size, void* d_ws, size_t ws_size, hipStream_t stream) {
  Params p;
  for (int i = 0; i < 14; ++i) p.in[i] = (const u16*)d_in[i];
  p.ws = (char*)d_ws; p.out = (u16*)d_out;
  fused<<<480, 512, 0, stream>>>(p);
}
```

```cpp
#include <hip/hip_runtime.h>
#include <stdint.h>

typedef unsigned short u16;
typedef unsigned int u32;
typedef short s16x4 __attribute__((ext_vector_type(4)));
typedef __bf16 bf16x8 __attribute__((ext_vector_type(8)));
typedef __bf16 bf16x2 __attribute__((ext_vector_type(2)));
typedef float f32x4 __attribute__((ext_vector_type(4)));
typedef float f32x2 __attribute__((ext_vector_type(2)));
typedef u32 u32x4 __attribute__((ext_vector_type(4)));
typedef u32 u32x2 __attribute__((ext_vector_type(2)));
typedef __amdgpu_buffer_rsrc_t rsrc_t;
#define LAS __attribute__((address_space(3)))
#define GAS __attribute__((address_space(1)))
#define DI __device__ __forceinline__

constexpr int S = 2048, D = 448, H = 16, DH = 28, NM = 256;

constexpr u32 WS_GEN = 0;
constexpr u32 WS_FLAG = 4096, FSTR = 128;
constexpr u32 F_B = 0;
constexpr u32 F_A = 224;
constexpr u32 NFLAG = F_A + 256;
constexpr u32 WS_XT = 1u << 20;
constexpr u32 WS_KVP = WS_XT + D * S * 4;
constexpr u32 WS_OF = WS_KVP + 16 * 16 * 4 * 1024;
constexpr u32 WS_YT = WS_OF + 128 * 16 * 1024;
constexpr u32 WS_GATE = WS_YT + D * S * 4;
constexpr u32 WS_END = WS_GATE + S * 4;
static_assert(WS_FLAG + NFLAG * FSTR <= WS_XT, "flags");
constexpr int MAXSPIN = 400000;
#define CODE_BYTES 8192

#define STAMP(i)
#define STAMP_DECL
#define STAMP_FLUSH(ws)

DI float bflo(u32 v) { return __uint_as_float(v << 16); }
DI float bfhi(u32 v) { return __uint_as_float(v & 0xffff0000u); }
DI u32 pk2(float lo, float hi) { f32x2 p = {lo, hi}; bf16x2 q = __builtin_convertvector(p, bf16x2); return __builtin_bit_cast(u32, q); }
DI float smul(float a, float b) { float r; asm("v_mul_f32 %0, %1, %2" : "=v"(r) : "v"(a), "v"(b)); return r; }
DI float rbf(float f) { return bflo(pk2(f, f)); }
DI f32x4 mfma16(u32x4 a, u32x4 b, f32x4 c) {
  return __builtin_amdgcn_mfma_f32_16x16x32_bf16(__builtin_bit_cast(bf16x8, a), __builtin_bit_cast(bf16x8, b), c, 0, 0, 0);
}
DI u32x2 trread(const char* lds_base, u32 off) {
  s16x4 t = __builtin_amdgcn_ds_read_tr16_b64_v4i16((LAS s16x4*)((LAS char*)lds_base + off));
  return __builtin_bit_cast(u32x2, t);
}
DI rsrc_t mkrs(const void* p, u32 bytes) { return __builtin_amdgcn_make_buffer_rsrc((void*)p, 0, bytes, 0x00020000); }
DI void dma16(rsrc_t r, char* lds_dst, u32 voff, u32 soff) { __builtin_amdgcn_raw_ptr_buffer_load_lds(r, (LAS void*)lds_dst, 16, voff, soff, 0, 0); }
DI void touch4(rsrc_t r, char* lds_dst, u32 voff, u32 soff) { __builtin_amdgcn_raw_ptr_buffer_load_lds(r, (LAS void*)lds_dst, 4, voff, soff, 0, 0); }
DI u32x4 mkrs4(const void* p, u32 bytes) { unsigned long long a = (unsigned long long)p; return (u32x4){(u32)a, (u32)(a >> 32) & 0xffffu, bytes, 0x00020000u}; }
DI u32 ldsaddr(const char* p) { return (u32)(uintptr_t)(LAS const char*)p; }
DI void adma16(u32x4 rs, u32 lds_dst, u32 voff, u32 soff) {
  asm volatile("s_mov_b32 m0, %0\n\ts_nop 0\n\tbuffer_load_dwordx4 %1, %2, %3 offen lds" ::"s"(lds_dst), "v"(voff), "s"(rs), "s"(soff) : "memory");
}
DI void adma16h(u32x4 rs, u32 lds_dst, u32 voff, u32 soff) {
  asm volatile("s_mov_b32 m0, %0\n\ts_mov_b32 exec_hi, 0\n\tbuffer_load_dwordx4 %1, %2, %3 offen lds\n\ts_mov_b32 exec_hi, -1" ::"s"(lds_dst), "v"(voff), "s"(rs), "s"(soff) : "memory");
}
DI u32 sload(const void* p) { return *(const __attribute__((address_space(4))) u32*)(unsigned long long)p; }
DI void atouch4(u32x4 rs, u32 lds_dst, u32 voff, u32 soff) {
  asm volatile("s_mov_b32 m0, %0\n\ts_nop 0\n\tbuffer_load_dword %1, %2, %3 offen lds" ::"s"(lds_dst), "v"(voff), "s"(rs), "s"(soff) : "memory");
}
DI u32x4 ald16(u32x4 rs, u32 voff, u32 soff) {
  u32x4 r; asm volatile("buffer_load_dwordx4 %0, %1, %2, %3 offen" : "=v"(r) : "v"(voff), "s"(rs), "s"(soff) : "memory"); return r;
}
template <int N> DI void waitv4(u32x4& a, u32x4& b, u32x4& c, u32x4& d) { asm volatile("s_waitcnt vmcnt(%4)" : "+v"(a), "+v"(b), "+v"(c), "+v"(d) : "i"(N) : "memory"); }
DI u32x4 ld16(rsrc_t r, u32 voff, u32 soff) { return __builtin_amdgcn_raw_buffer_load_b128(r, voff, soff, 0); }
DI u32x4 ld16c(rsrc_t r, u32 voff, u32 soff) { return __builtin_amdgcn_raw_buffer_load_b128(r, voff, soff, 16); }
DI u32x2 ld8c(rsrc_t r, u32 voff, u32 soff) { return __builtin_amdgcn_raw_buffer_load_b64(r, voff, soff, 16); }
DI u32 ld4c(rsrc_t r, u32 voff, u32 soff) { return __builtin_amdgcn_raw_buffer_load_b32(r, voff, soff, 16); }
DI u32 ld2c(rsrc_t r, u32 voff, u32 soff) { return __builtin_amdgcn_raw_buffer_load_b16(r, voff, soff, 16); }
DI void st16c(u32x4 v, rsrc_t r, u32 voff, u32 soff) { __builtin_amdgcn_raw_buffer_store_b128(v, r, voff, soff, 16); }
DI void st4c(u32 v, rsrc_t r, u32 voff, u32 soff) { __builtin_amdgcn_raw_buffer_store_b32(v, r, voff, soff, 16); }
#define WAIT_L(n) asm volatile("s_waitcnt lgkmcnt(" #n ")" ::: "memory")
#define BAR() __builtin_amdgcn_s_barrier()
#define CFENCE() asm volatile("" ::: "memory")
template <int N> DI void waitv() { asm volatile("s_waitcnt vmcnt(%0)" ::"i"(N) : "memory"); }
DI void bar_l() { WAIT_L(0); BAR(); CFENCE(); }

DI u32 flag_off(u32 idx) { return WS_FLAG + idx * FSTR; }
DI void publish(char* ws, u32 idx, u32 tag, int lane) {
  waitv<0>();
  if (lane == 0) __hip_atomic_store((u32*)(ws + flag_off(idx)), tag, __ATOMIC_RELAXED, __HIP_MEMORY_SCOPE_AGENT);
}
DI void pollf(char* ws, u32 idx, u32 tag) {
  const u32* f = (const u32*)(ws + flag_off(idx));
#pragma unroll 1
  for (int it = 0; it < MAXSPIN; ++it) {
    u32 v = __hip_atomic_load(f, __ATOMIC_RELAXED, __HIP_MEMORY_SCOPE_AGENT);
    if (__all(v == tag)) break;
    __builtin_amdgcn_s_sleep(1);
  }
  CFENCE();
}

DI float red_max_g(float x) {
  u32 u = __float_as_uint(x);
  auto r = __builtin_amdgcn_permlane16_swap(u, u, false, false);
  x = fmaxf(__uint_as_float(r[0]), __uint_as_float(r[1]));
  u = __float_as_uint(x);
  auto r2 = __builtin_amdgcn_permlane32_swap(u, u, false, false);
  return fmaxf(__uint_as_float(r2[0]), __uint_as_float(r2[1]));
}
DI float red_sum_g(float x) {
  u32 u = __float_as_uint(x);
  auto r = __builtin_amdgcn_permlane16_swap(u, u, false, false);
  x = __uint_as_float(r[0]) + __uint_as_float(r[1]);
  u = __float_as_uint(x);
  auto r2 = __builtin_amdgcn_permlane32_swap(u, u, false, false);
  return __uint_as_float(r2[0]) + __uint_as_float(r2[1]);
}
template <int CTRL> DI float dpp_add(float x) {
  return x + __uint_as_float(__builtin_amdgcn_update_dpp(0, __float_as_uint(x), CTRL, 0xf, 0xf, false));
}
DI float red_sum_all(float x) {
  x = dpp_add<0x128>(x); x = dpp_add<0x124>(x); x = dpp_add<0x122>(x); x = dpp_add<0x121>(x);
  return red_sum_g(x);
}

#define BPRIO2 0
#define APRIO2 2
#define APRIO 0
#define BPRIO 2
DI void dual_softmax(const f32x4 (&pacc)[2], u32x4& pos, u32x4& neg) {
  constexpr float L2E = 1.4426950408889634f;
  float p[7];
  {
    u32 a = pk2(pacc[0][0], pacc[0][1]), b = pk2(pacc[0][2], pacc[0][3]), c = pk2(pacc[1][0], pacc[1][1]), d = pk2(pacc[1][2], pacc[1][2]);
    p[0] = bflo(a); p[1] = bfhi(a); p[2] = bflo(b); p[3] = bfhi(b); p[4] = bflo(c); p[5] = bfhi(c); p[6] = bflo(d);
  }
  float mx = fmaxf(fmaxf(fmaxf(p[0], p[1]), fmaxf(p[2], p[3])), fmaxf(fmaxf(p[4], p[5]), p[6]));
  float mn = fminf(fminf(fminf(p[0], p[1]), fminf(p[2], p[3])), fminf(fminf(p[4], p[5]), p[6]));
  mx = red_max_g(mx);
  mn = -red_max_g(-mn);
  float ep[7], en[7];
  float sp = 0.f, sn = 0.f;
  const float mxl = mx * L2E, mnl = mn * L2E;
#pragma unroll
  for (int i = 0; i < 7; ++i) {
    ep[i] = __builtin_amdgcn_exp2f(__builtin_fmaf(p[i], L2E, -mxl));
    en[i] = __builtin_amdgcn_exp2f(__builtin_fmaf(p[i], -L2E, mnl));
    sp += ep[i]; sn += en[i];
  }
  sp = red_sum_g(sp); sn = red_sum_g(sn);
  const float rp = __builtin_amdgcn_rcpf(sp), rn = __builtin_amdgcn_rcpf(sn);
#pragma unroll
  for (int i = 0; i < 3; ++i) {
    pos[i] = pk2(ep[2 * i] * rp, ep[2 * i + 1] * rp);
    neg[i] = pk2(en[2 * i] * rn, en[2 * i + 1] * rn);
  }
  pos[3] = pk2(ep[6] * rp, 0.f); neg[3] = pk2(en[6] * rn, 0.f);
}
DI float acc_sel(u32 v, u32 sel, float a) { return __builtin_amdgcn_fdot2_f32_bf16(__builtin_bit_cast(bf16x2, v), __builtin_bit_cast(bf16x2, sel), a, false); }
DI bool tag_ok(u32 v3, u32 t16) { return __all((v3 >> 16) == t16); }

struct Params { const u16* in[14]; char* ws; u16* out; };

constexpr int A_X = 0, A_W = 32768, A_WPHI = 73728, A_GW = 75776, A_VT = 16384, A_Q6 = 24576, A_KVB = 76800, A_RED = 65536;
constexpr int LDS_BYTES = 81920;
constexpr int WROW = 3 * D * 2;
constexpr int WCH = 64 * WROW;

DI void roleA(const Params& p, int x8, int j, char* lds) {
  const int tid = threadIdx.x, lane = tid & 63, w = __builtin_amdgcn_readfirstlane(tid >> 6);
  const int g = lane >> 4, l15 = lane & 15, q4 = l15 >> 2, pp = lane & 3;
  const int h = 4 * (x8 & 3) + (j & 3), tile = 8 * (x8 >> 2) + (j >> 2);
  const int rot = (2 * (h & 3) + (tile & 7)) % 7;
  char* ws = p.ws;
  STAMP_DECL
  STAMP(0);
  __builtin_amdgcn_s_setprio(3);
  const u32x4 rX = mkrs4(p.in[0], S * D * 2), rW = mkrs4(p.in[1], D * 3 * D * 2);
  const rsrc_t rWS = mkrs(ws, WS_END);
  const u32 lb = ldsaddr(lds);
  const u32 gen = sload(ws + WS_GEN);
  const int tok0 = tile * 128 + w * 16;
  u32 xvo[2];
#pragma unroll
  for (int i = 0; i < 2; ++i) { int row = 8 * i + (lane >> 3); xvo[i] = (tok0 + row) * (D * 2) + (((lane & 7) ^ ((row >> 1) & 7)) * 16); }
  u32 wvo;
  { int row = 16 * (w & 3) + (lane >> 2); int cs = (lane & 3) ^ (2 * ((row >> 3) & 1)); wvo = row * WROW + h * (DH * 2) + (cs < 3 ? cs * 16 : 40); }
  auto cidx = [&](int i) { int c = rot + i; return c >= 7 ? c - 7 : c; };
  auto issueX = [&](int i) {
    const u32 so = cidx(i) * 128, dst = lb + A_X + (i & 1) * 16384 + w * 2048;
    adma16(rX, dst, xvo[0], so); adma16(rX, dst + 1024, xvo[1], so);
  };
  auto issueW = [&](int v) {
    const u32 dst = lb + A_W + (v % 5) * 8192 + w * 1024;
    if (v < 7) adma16(rW, dst, wvo, cidx(v) * WCH + (1 + (w >> 2)) * (D * 2));
    else adma16(rW, dst, wvo, cidx(2 * (v - 7) + (w >> 2)) * WCH);
  };
  if (w < 2) adma16(mkrs4(p.in[2], H * DH * DH * 2), lb + A_WPHI + w * 1024, lane * 16, h * (DH * DH * 2) + w * 1024);
  else if (w == 2) { int c = rot + (lane >> 3); c = c >= 7 ? c - 7 : c; adma16(mkrs4(p.in[13], D * 2), lb + A_GW, lane < 56 ? c * 128 + (lane & 7) * 16 : 4096, 0); }
  issueW(0); issueX(0);
  __builtin_amdgcn_s_setprio(2);
  issueW(1); issueX(1);
  __builtin_amdgcn_s_setprio(1);
  issueW(2);
  __builtin_amdgcn_s_setprio(APRIO);
  __builtin_amdgcn_sched_barrier(0);
  STAMP(1);
  f32x4 ak[2] = {{0, 0, 0, 0}, {0, 0, 0, 0}}, av[2] = {{0, 0, 0, 0}, {0, 0, 0, 0}};
  u32 xoff[2];
#pragma unroll
  for (int ks = 0; ks < 2; ++ks) xoff[ks] = w * 2048 + l15 * 128 + (((4 * ks + g) ^ ((l15 >> 1) & 7)) * 16);
  u32 woff[2];
#pragma unroll
  for (int ct = 0; ct < 2; ++ct) woff[ct] = (8 * g + q4) * 64 + ((ct ^ (g & 1)) * 32) + pp * 8;
  const u32 goff = w * 2048 + h * 128 + (((lane >> 3) ^ ((h >> 1) & 7)) * 16) + (lane & 7) * 2;
  float gs = 0.f;
  u32x4 xf[14];
  u32x4 wf0[2][2], wf1[2][2];
  u32 gx, gw;
  auto readX = [&](int i) {
    const char* xs = lds + A_X + (i & 1) * 16384;
    xf[2 * i] = *(const u32x4*)(xs + xoff[0]); xf[2 * i + 1] = *(const u32x4*)(xs + xoff[1]);
    gx = *(const u16*)(xs + goff); gw = *(const u16*)(lds + A_GW + i * 128 + lane * 2);
  };
  auto readW = [&](int i, int ks, u32x4 (&wf)[2][2]) {
    const char* wsb = lds + A_W + (i % 5) * 8192;
#pragma unroll
    for (int r = 0; r < 2; ++r)
#pragma unroll
      for (int ct = 0; ct < 2; ++ct) {
        u32x2 a = trread(wsb, woff[ct] + r * 4096 + ks * 2048);
        u32x2 b = trread(wsb, woff[ct] + r * 4096 + ks * 2048 + 256);
        wf[r][ct] = (u32x4){a[0], a[1], b[0], b[1]};
      }
  };
  auto mm = [&](const u32x4& x, const u32x4 (&wf)[2][2]) {
#pragma unroll
    for (int ct = 0; ct < 2; ++ct) { ak[ct] = mfma16(wf[0][ct], x, ak[ct]); av[ct] = mfma16(x, wf[1][ct], av[ct]); }
  };
  waitv<3>();
  BAR();
  CFENCE();
  readX(0); readW(0, 0, wf0);
  __builtin_amdgcn_sched_barrier(0);
  readW(0, 1, wf1);
  __builtin_amdgcn_sched_barrier(0);
  WAIT_L(8);
  issueX(2); issueW(3);
#pragma unroll
  for (int i = 0; i < 7; ++i) {
    __builtin_amdgcn_sched_barrier(0);
    const u32 gx0 = gx, gw0 = gw;
    mm(xf[2 * i], wf0);
    gs += rbf(smul(bflo(gx0), bflo(gw0)));
    asm volatile("" : "+v"(gs));
    __builtin_amdgcn_sched_barrier(0);
    if (i < 6) {
      if (i == 0 || i == 3 || i == 4) waitv<4>(); else if (i == 5) { if (w < 4) waitv<3>(); else waitv<2>(); } else waitv<5>();
      readX(i + 1); readW(i + 1, 0, wf0);
    }
    __builtin_amdgcn_sched_barrier(0);
    mm(xf[2 * i + 1], wf1);
    __builtin_amdgcn_sched_barrier(0);
    if (i == 1 && w == 7) {
      u32x4 wph[2];
#pragma unroll
      for (int ft = 0; ft < 2; ++ft) {
        int f = 7 * q4 + 4 * ft + pp; f = f > 27 ? 27 : f;
        const char* wp = lds + A_WPHI + f * 2;
        const int d0 = 4 * g, d1 = g < 2 ? 16 + 4 * g : 24;
        u32 e[8];
#pragma unroll
        for (int jj = 0; jj < 4; ++jj) { e[jj] = *(const u16*)(wp + (d0 + jj) * 56); e[4 + jj] = *(const u16*)(wp + (d1 + jj) * 56); }
        wph[ft] = (u32x4){e[0] | (e[1] << 16), e[2] | (e[3] << 16), g == 2 ? 0u : (e[4] | (e[5] << 16)), g == 2 ? 0u : (e[6] | (e[7] << 16))};
      }
      CFENCE();
      *(u32x4*)(lds + A_WPHI + lane * 16) = wph[0]; *(u32x4*)(lds + A_WPHI + 1024 + lane * 16) = wph[1];
      WAIT_L(0);
    }
    if (i < 6) {
      readW(i + 1, 1, wf1);
      __builtin_amdgcn_sched_barrier(0);
      WAIT_L(8);
      if (i + 3 < 7) issueX(i + 3);
      if (i == 0) issueW(4);
      if (i < 5) {
        if (i < 3) waitv<6>();
        BAR();
        CFENCE();
        issueW(i + 5);
        if (i == 4 && w < 4) adma16(rW, lb + A_Q6 + w * 1024, wvo, cidx(6) * WCH);
      }
    }
  }
  __builtin_amdgcn_s_setprio(APRIO2);
  STAMP(2);
  const u32 tag = gen + 1u, t16 = tag & 0xffffu;
  char* phk = lds + A_X; char* vt = lds + A_VT;
  {
    const int tl = w * 16 + l15;
    u32x4 kb = {pk2(ak[0][0], ak[0][1]), pk2(ak[0][2], ak[0][3]), pk2(ak[1][0], ak[1][1]), pk2(ak[1][2], ak[1][3])};
    f32x4 z4 = {0, 0, 0, 0}; f32x4 pk[2];
    pk[0] = mfma16(*(const u32x4*)(lds + A_WPHI + lane * 16), kb, z4); pk[1] = mfma16(*(const u32x4*)(lds + A_WPHI + 1024 + lane * 16), kb, z4);
    u32x4 pos, neg; dual_softmax(pk, pos, neg);
    const int sw = ((tl >> 1) & 1) | (((tl >> 3) & 1) << 1);
    char* rowp = phk + tl * 128 + g * 8;
    *(u32x2*)(rowp + ((0 ^ sw) * 32)) = (u32x2){pos[0], pos[1]};
    *(u32x2*)(rowp + ((1 ^ sw) * 32)) = (u32x2){pos[2], pos[3]};
    *(u32x2*)(rowp + ((2 ^ sw) * 32)) = (u32x2){neg[0], neg[1]};
    *(u32x2*)(rowp + ((3 ^ sw) * 32)) = (u32x2){neg[2], neg[3]};
  }
  STAMP(3);
  {
#pragma unroll
    for (int ct = 0; ct < 2; ++ct) {
      int d = ct == 0 ? l15 : (l15 < 8 ? 16 + l15 : 12 + l15);
      int G = (d * 37) >> 8, e = d - 7 * G;
      int rho = e < 4 ? 4 * G + e : 12 + 4 * G + e;
      f32x4 v = av[ct];
      if (ct == 1 && (l15 >> 2) == 2) { rho = pp == 0 ? 27 : (pp == 1 ? 19 : (pp == 2 ? 23 : 31)); if (pp == 0) v = (f32x4){1.f, 1.f, 1.f, 1.f}; }
      *(u32x2*)(vt + rho * 256 + (((2 * w + (g >> 1)) ^ (rho & 15)) * 16) + (g & 1) * 8) = (u32x2){pk2(v[0], v[1]), pk2(v[2], v[3])};
    }
  }
  bar_l();
  STAMP(4);
  if (w < 4) {
    const int sg = w >> 1, dt = w & 1;
    f32x4 t0 = {0, 0, 0, 0}, t1 = t0;
#pragma unroll
    for (int ks = 0; ks < 4; ++ks) {
      u32x4 af[2];
#pragma unroll
      for (int ft = 0; ft < 2; ++ft) {
        u32x2 a[2];
#pragma unroll
        for (int t = 0; t < 2; ++t) {
          int row = 32 * ks + 8 * g + 4 * t + q4;
          int sw = ((row >> 1) & 1) | (((row >> 3) & 1) << 1);
          a[t] = trread(phk, row * 128 + (((2 * sg + ft) ^ sw) * 32) + pp * 8);
        }
        af[ft] = (u32x4){a[0][0], a[0][1], a[1][0], a[1][1]};
      }
      u32x4 bf = *(const u32x4*)(vt + (16 * dt + l15) * 256 + (((4 * ks + g) ^ l15) * 16));
      t0 = mfma16(af[0], bf, t0); t1 = mfma16(af[1], bf, t1);
    }
    u32x4 un = {pk2(t0[0], t0[1]), pk2(t0[2], t0[3]), pk2(t1[0], t1[1]), (pk2(t1[2], t1[2]) & 0xffffu) | (t16 << 16)};
    st16c(un, rWS, (((h * 16 + tile) * 4 + w) * 64 + lane) * 16, WS_KVP);
    waitv<1>();
  } else waitv<0>();
  STAMP(5);
  BAR();
  CFENCE();
  if (tid == 511) __hip_atomic_store((u32*)(ws + flag_off(F_A + blockIdx.x)), tag, __ATOMIC_RELAXED, __HIP_MEMORY_SCOPE_AGENT);
  u32x4 pqp, pqn;
  u32x4 pv[16];
  const u32 pvo = ((h * 16 * 4 + (w & 3)) * 64 + lane) * 16;
  {
    f32x4 aq[2] = {{0, 0, 0, 0}, {0, 0, 0, 0}};
#pragma unroll
    for (int i = 0; i < 7; ++i) {
      const char* qb = lds + (i == 6 ? A_Q6 : A_W + ((7 + (i >> 1)) % 5) * 8192 + (i & 1) * 4096);
#pragma unroll
      for (int ks = 0; ks < 2; ++ks)
#pragma unroll
        for (int ct = 0; ct < 2; ++ct) {
          u32x2 a = trread(qb, woff[ct] + ks * 2048);
          u32x2 b = trread(qb, woff[ct] + ks * 2048 + 256);
          aq[ct] = mfma16((u32x4){a[0], a[1], b[0], b[1]}, xf[2 * i + ks], aq[ct]);
        }
    }
    const u32x4 wq0 = *(const u32x4*)(lds + A_WPHI + lane * 16), wq1 = *(const u32x4*)(lds + A_WPHI + 1024 + lane * 16);
    bar_l();
    if (w < 4) {
      const u32 r0 = (tile & 3) * 4;
#pragma unroll
      for (int t = 0; t < 16; ++t) pv[t] = ld16c(rWS, pvo, WS_KVP + ((t + r0) & 15) * 4096);
    }
    __builtin_amdgcn_sched_barrier(0);
    u32x4 qb = {pk2(aq[0][0], aq[0][1]), pk2(aq[0][2], aq[0][3]), pk2(aq[1][0], aq[1][1]), pk2(aq[1][2], aq[1][3])};
    f32x4 z4 = {0, 0, 0, 0}; f32x4 pq[2];
    pq[0] = mfma16(wq0, qb, z4); pq[1] = mfma16(wq1, qb, z4);
    dual_softmax(pq, pqp, pqn);
    __builtin_amdgcn_sched_barrier(0);
    const bool fin0 = j < 28;
    if (w >= 4 && fin0) {
      const u32x4 rWO = mkrs4(p.in[3], D * D * 2);
      const int jr = lane >> 3;
      const u32 vo = (jr < 7 ? jr : 6) * (D * 2) + (j % 7) * 128 + (((lane & 7) ^ (2 * (((jr >> 1) & 1) | ((w & 1) << 1)))) * 16);
#pragma unroll
      for (int m = 0; m < 16; ++m) adma16(rWO, lb + ((w - 4) + 4 * m) * 1024, vo, ((w - 4) + 4 * m) * (7 * D * 2));
    }
  }
  STAMP(6);
  const bool fin = j < 28;
  const int tt = 4 * x8 + j / 7, cg = j % 7, c0 = cg * 64;
  const int rt = w >> 1, kh = w & 1;
  const int ftok = tt * 64 + rt * 16 + l15;
  if (w < 4) {
    u32 pend = 15;
#pragma unroll 1
    for (int it = 0; it < MAXSPIN; ++it) {
#pragma unroll
      for (int gq = 0; gq < 4; ++gq)
        if (pend & (1u << gq)) {
          const u32 m = (pv[4 * gq][3] ^ (t16 << 16)) | (pv[4 * gq + 1][3] ^ (t16 << 16)) | (pv[4 * gq + 2][3] ^ (t16 << 16)) | (pv[4 * gq + 3][3] ^ (t16 << 16));
          if (__all(m < 0x10000u)) pend &= ~(1u << gq);
        }
      if (!pend) break;
      __builtin_amdgcn_s_sleep(2);
#pragma unroll
      for (int gq = 0; gq < 4; ++gq)
        if (pend & (1u << gq)) {
#pragma unroll
          for (int t = 0; t < 4; ++t) pv[4 * gq + t] = ld16c(rWS, pvo, WS_KVP + ((4 * gq + t + (tile & 3) * 4) & 15) * 4096);
        }
    }
    float s[7] = {0, 0, 0, 0, 0, 0, 0};
    u32 selo = 0x00003f80u, sehi = 0x3f800000u;
    asm volatile("" : "+s"(selo), "+s"(sehi));
#pragma unroll
    for (int t = 0; t < 16; ++t) {
      s[0] = acc_sel(pv[t][0], selo, s[0]); s[1] = acc_sel(pv[t][0], sehi, s[1]); s[2] = acc_sel(pv[t][1], selo, s[2]); s[3] = acc_sel(pv[t][1], sehi, s[3]);
      s[4] = acc_sel(pv[t][2], selo, s[4]); s[5] = acc_sel(pv[t][2], sehi, s[5]); s[6] += bflo(pv[t][3]);
    }
    *(u32x4*)(lds + A_KVB + w * 1024 + lane * 16) = (u32x4){pk2(s[0], s[1]), pk2(s[2], s[3]), pk2(s[4], s[5]), pk2(s[6], 0.f)};
  }
  const float gate = rbf(__builtin_amdgcn_rcpf(1.f + __expf(-rbf(red_sum_all(gs)))));
  bar_l();
  STAMP(7);
  {
    f32x4 o[2];
#pragma unroll
    for (int dt = 0; dt < 2; ++dt) {
      const u32x4 kp = *(const u32x4*)(lds + A_KVB + dt * 1024 + lane * 16), kn = *(const u32x4*)(lds + A_KVB + (2 + dt) * 1024 + lane * 16);
      o[dt] = mfma16(kp, pqp, (f32x4){0, 0, 0, 0});
      o[dt] = mfma16(kn, pqn, o[dt]);
    }
    u32 du = __float_as_uint(o[1][3]);
    auto r1 = __builtin_amdgcn_permlane32_swap(du, du, false, false);
    u32 d1 = r1[1];
    auto r2 = __builtin_amdgcn_permlane16_swap(d1, d1, false, false);
    float den = __uint_as_float(r2[0]);
    den = rbf(rbf(den) + 1e-6f);
    const float rd = __builtin_amdgcn_rcpf(den);
    const u32 n0 = pk2(o[0][0], o[0][1]), n1 = pk2(o[0][2], o[0][3]), n2 = pk2(o[1][0], o[1][1]), n3 = pk2(o[1][2], o[1][2]);
    u32x4 of = {pk2(bflo(n0) * rd, bfhi(n0) * rd), pk2(bflo(n1) * rd, bfhi(n1) * rd), pk2(bflo(n2) * rd, bfhi(n2) * rd), (pk2(bflo(n3) * rd, 0.f) & 0xffffu) | (t16 << 16)};
    const int rt16 = tile * 8 + w;
    st16c(of, rWS, ((rt16 * 16 + h) * 64 + lane) * 16, WS_OF);
    if (lane == 0) st4c((__float_as_uint(gate) >> 16) | (t16 << 16), rWS, (tok0 + h) * 4, WS_GATE);
  }
  STAMP(8);
  if (fin) {
    const int rt16 = tt * 4 + rt;
    u32x2 xin[2], dsk[2];
#pragma unroll
    for (int m = 0; m < 2; ++m) {
      xin[m] = *(const u32x2*)(p.in[0] + (size_t)ftok * D + c0 + 32 * kh + 16 * m + 4 * g);
      dsk[m] = *(const u32x2*)(p.in[12] + c0 + 32 * kh + 16 * m + 4 * g);
    }
    u32x4 of[8];
    u32 yv[2][4], gv;
    const u32 ovo = ((rt16 * 16 + 8 * kh) * 64 + lane) * 16;
    const u32 yvo = (c0 + 32 * kh + 4 * g) * (S * 4) + ftok * 4;
    u32 pend = 15;
#pragma unroll
    for (int t = 0; t < 8; ++t) of[t] = ld16c(rWS, ovo, WS_OF + t * 1024);
    gv = ld4c(rWS, ftok * 4, WS_GATE);
#pragma unroll
    for (int m = 0; m < 2; ++m)
#pragma unroll
      for (int i = 0; i < 4; ++i) yv[m][i] = ld4c(rWS, yvo, WS_YT + (16 * m + i) * (S * 4));
    const u32 tw = t16 << 16;
#pragma unroll 1
    for (int it = 0; it < MAXSPIN; ++it) {
#pragma unroll
      for (int gq = 0; gq < 2; ++gq)
        if (pend & (1u << gq)) {
          const u32 m = (of[4 * gq][3] ^ tw) | (of[4 * gq + 1][3] ^ tw) | (of[4 * gq + 2][3] ^ tw) | (of[4 * gq + 3][3] ^ tw);
          if (__all(m < 0x10000u)) pend &= ~(1u << gq);
        }
      if ((pend & 4) && __all((gv ^ tw) < 0x10000u)) pend &= ~4u;
      if (pend & 8) {
        const u32 m = (yv[0][0] ^ tw) | (yv[0][1] ^ tw) | (yv[0][2] ^ tw) | (yv[0][3] ^ tw) | (yv[1][0] ^ tw) | (yv[1][1] ^ tw) | (yv[1][2] ^ tw) | (yv[1][3] ^ tw);
        if (__all(m < 0x10000u)) pend &= ~8u;
      }
      if (!pend) break;
      if (pend == 8) __builtin_amdgcn_s_sleep(8); else __builtin_amdgcn_s_sleep(2);
#pragma unroll
      for (int gq = 0; gq < 2; ++gq)
        if (pend & (1u << gq)) {
#pragma unroll
          for (int t = 0; t < 4; ++t) of[4 * gq + t] = ld16c(rWS, ovo, WS_OF + (4 * gq + t) * 1024);
        }
      if (pend & 4) gv = ld4c(rWS, ftok * 4, WS_GATE);
      if (pend & 8) {
#pragma unroll
        for (int m = 0; m < 2; ++m)
#pragma unroll
          for (int i = 0; i < 4; ++i) yv[m][i] = ld4c(rWS, yvo, WS_YT + (16 * m + i) * (S * 4));
      }
    }
    STAMP(9);
    waitv<0>();
    BAR();
    CFENCE();
    u32 wa[4];
    { const int s = 2 * (((q4 >> 1) & 1) | ((g & 1) << 1));
#pragma unroll
      for (int ct = 0; ct < 4; ++ct) wa[ct] = kh * 32768 + (8 * g + q4) * 128 + (((2 * ct) ^ s) * 16) + pp * 8; }
    f32x4 acc[4];
#pragma unroll
    for (int ct = 0; ct < 4; ++ct) acc[ct] = (f32x4){0, 0, 0, 0};
#pragma unroll
    for (int hh = 0; hh < 8; ++hh) {
      u32x4 ob = of[hh]; ob[3] &= 0xffffu;
#pragma unroll
      for (int ct = 0; ct < 4; ++ct) {
        u32x2 a = trread(lds, wa[ct] + hh * 4096);
        u32x2 b = trread(lds, wa[ct] + hh * 4096 + 512);
        acc[ct] = mfma16((u32x4){a[0], a[1], b[0], b[1]}, ob, acc[ct]);
      }
    }
    STAMP(10);
    const f32x4 c0_ = acc[0], c1_ = acc[1], c2_ = acc[2], c3_ = acc[3];
    {
      f32x4* rw = (f32x4*)(lds + A_RED + w * 2048) + lane;
      rw[0] = kh ? c0_ : c2_; rw[64] = kh ? c1_ : c3_;
    }
    const f32x4 keep0 = kh ? c2_ : c0_, keep1 = kh ? c3_ : c1_;
    bar_l();
    const f32x4* rr = (const f32x4*)(lds + A_RED + (w ^ 1) * 2048) + lane;
    const float gg = bflo(gv), og = rbf(1.f - gg);
    u32 res[2][2];
#pragma unroll
    for (int m = 0; m < 2; ++m) {
      f32x4 a = rr[m * 64];
      a += m ? keep1 : keep0;
      const float xx[4] = {bflo(xin[m][0]), bfhi(xin[m][0]), bflo(xin[m][1]), bfhi(xin[m][1])};
      const float ds[4] = {bflo(dsk[m][0]), bfhi(dsk[m][0]), bflo(dsk[m][1]), bfhi(dsk[m][1])};
      float r[4];
#pragma unroll
      for (int i = 0; i < 4; ++i) {
        float av_ = rbf(a[i]);
        float b = rbf(bflo(yv[m][i]) + rbf(smul(xx[i], ds[i])));
        r[i] = rbf(smul(gg, av_)) + rbf(smul(og, b));
      }
      res[m][0] = pk2(r[0], r[1]); res[m][1] = pk2(r[2], r[3]);
    }
    u32x4 ov;
    {
      auto s0 = __builtin_amdgcn_permlane32_swap(res[0][0], res[1][0], false, false);
      auto s1 = __builtin_amdgcn_permlane32_swap(res[0][1], res[1][1], false, false);
      auto t0 = __builtin_amdgcn_permlane16_swap(s0[0], s0[1], false, false);
      auto t1 = __builtin_amdgcn_permlane16_swap(s1[0], s1[1], false, false);
      ov = (u32x4){t0[0], t1[0], t0[1], t1[1]};
    }
    *(u32x4*)(p.out + (size_t)ftok * D + c0 + 32 * kh + 8 * g) = ov;
    STAMP(11);
  }
  else if (x8 == 0 && j == 31) {
    pollf(ws, tid < 480 ? tid : 0, tag);
    BAR();
    if (tid == 0) __hip_atomic_store((u32*)(ws + WS_GEN), tag, __ATOMIC_RELAXED, __HIP_MEMORY_SCOPE_AGENT);
  }
  STAMP_FLUSH(ws);
}

DI f32x2 cmul(f32x2 a, f32x2 b) {
  f32x2 m, r;
  asm("v_pk_mul_f32 %0, %1, %2 op_sel:[1,0] op_sel_hi:[1,1]" : "=v"(m) : "v"(b), "v"(a));
  asm("v_pk_fma_f32 %0, %1, %2, %3 op_sel:[0,0,1] op_sel_hi:[1,0,0] neg_lo:[0,0,1]" : "=v"(r) : "v"(a), "v"(b), "v"(m));
  return r;
}
DI f32x2 cexp_pow(float mr, float pr, float a) {
  float m = __builtin_amdgcn_exp2f(mr * a);
  float t = __builtin_amdgcn_fractf(pr * a);
  return (f32x2){m * __builtin_amdgcn_cosf(t), m * __builtin_amdgcn_sinf(t)};
}
constexpr int KG_ROW = 272, KG_SLAB = 16 * KG_ROW;
constexpr int B_UC = 34816, UCH = 16656, B_KR = 68128, B_KTAB = 77344, B_DUM = 80416, B_TIMG = B_KR, B_RED = 0;
constexpr int UC0 = 8, UC1 = 4176, UC2 = 8336, UC3 = 12496;

DI void roleB(const Params& p, int x8, int jb, char* lds) {
  const int tid = threadIdx.x, lane = tid & 63, w = __builtin_amdgcn_readfirstlane(tid >> 6);
  const int g = lane >> 4, l15 = lane & 15;
  char* ws = p.ws;
  STAMP_DECL
  STAMP(0);
  __builtin_amdgcn_s_setprio(3);
  const rsrc_t rWS = mkrs(ws, WS_END);
  const int tt = 4 * x8 + jb / 7, cg = jb % 7, ch0 = 2 * (x8 * 28 + jb);
  const u32 gen = sload(ws + WS_GEN);
  if (w == 7) {
    const u32x4 rc = mkrs4((const void*)(__builtin_amdgcn_s_getpc() & ~(unsigned long long)127), CODE_BYTES);
#pragma unroll
    for (int i = 0; i < (CODE_BYTES + 8191) / 8192; ++i) atouch4(rc, ldsaddr(lds) + B_DUM, lane * 128, i * 8192);
  } else if (w >= 5) {
    atouch4(mkrs4(p.in[3], D * D * 2), ldsaddr(lds) + B_DUM, lane * 128, (jb * 2 + (w - 5)) * 7168);
  }
  const u32x4 xv = *(const u32x4*)(p.in[0] + (size_t)(tt * 64 + (tid >> 3)) * D + cg * 64 + (tid & 7) * 8);
  const int d = ch0 + (w >> 2);
  const int mo = d * NM + (w & 3) * 64 + lane;
  const u32 are = p.in[5][mo], aim = p.in[6][mo], bre = p.in[7][mo], bim = p.in[8][mo], cre = p.in[9][mo], cim = p.in[10][mo];
  const u32 ldt = p.in[11][d];
  __builtin_amdgcn_sched_barrier(0);
  const u32 tag = gen + 1u, tw = (tag & 0xffffu) << 16;
  {
    char* img = lds + B_TIMG;
    u32* dst = (u32*)(img + (tid >> 3) * 132 + (tid & 7) * 16);
    dst[0] = xv[0]; dst[1] = xv[1]; dst[2] = xv[2]; dst[3] = xv[3];
    bar_l();
    const int chl = tid >> 3, oc = tid & 7;
    u32 o[8];
#pragma unroll
    for (int jj = 0; jj < 8; ++jj) o[jj] = *(const u16*)(img + (8 * oc + jj) * 132 + chl * 2) | tw;
    st16c((u32x4){o[0], o[1], o[2], o[3]}, rWS, (cg * 64 + chl) * (S * 4) + tt * 256 + oc * 32, WS_XT);
    st16c((u32x4){o[4], o[5], o[6], o[7]}, rWS, (cg * 64 + chl) * (S * 4) + tt * 256 + oc * 32 + 16, WS_XT);
    if (tid == 0) __hip_atomic_store((u32*)(ws + flag_off(F_B + x8 * 28 + jb)), tag, __ATOMIC_RELAXED, __HIP_MEMORY_SCOPE_AGENT);
  }
  __builtin_amdgcn_s_setprio(BPRIO);
  STAMP(1);
  char* slab = lds + w * KG_SLAB;
  const int cch = tid >> 8, cq = tid & 255;
  u32x4 ua, ub, uc;
  const u32 uvo = (ch0 + cch) * (S * 4) + cq * 32;
  f32x4 acc[3];
  {
    const float dt = rbf(__expf(bflo(ldt)));
    const float ar = dt * bflo(are), ai = dt * bflo(aim), br = dt * bflo(bre), bi = dt * bflo(bim), cr = bflo(cre), ci = bflo(cim);
    const float mr = ar * 1.4426950408889634f, pr = ai * 0.15915494309189535f;
    f32x2 W = {cr * br - ci * bi, -(cr * bi + ci * br)};
    f32x2 z1c = cexp_pow(mr, -pr, 1.f);
    f32x2 z8 = cexp_pow(mr, pr, 8.f);
    f32x2 a128 = cexp_pow(mr, -pr, 128.f);
    asm volatile("s_mov_b32 m0, %0" ::"s"(ldsaddr(slab)) : "memory");
#define WROWT(r, v) do { const f32x2 v_ = (v); asm volatile("ds_write_addtid_b32 %0 offset:%1" ::"v"(pk2(v_.x, v_.y)), "i"((r) * KG_ROW) : "memory"); } while (0)
    const char* rd = slab + l15 * KG_ROW + g * 16;
    u32x4 yf[4];
    {
      f32x2 y[16];
      y[1] = z8;
      f32x2 y1c = z8; asm volatile("" : "+v"(y1c));
      y[2] = cmul(y[1], y1c); y[3] = cmul(y[2], y[1]); y[4] = cmul(y[3], y[1]);
      y[5] = cmul(y[4], y[1]); y[6] = cmul(y[4], y[2]); y[7] = cmul(y[4], y[3]); y[8] = cmul(y[5], y[3]);
#pragma unroll
      for (int c = 9; c < 16; ++c) y[c] = cmul(y[8], y[c - 8]);
      WROWT(0, ((f32x2){1.f, 0.f}));
#pragma unroll
      for (int c = 1; c < 16; ++c) WROWT(c, y[c]);
    }
#pragma unroll
    for (int ks = 0; ks < 4; ++ks) yf[ks] = *(const u32x4*)(rd + ks * 64);
    CFENCE();
    f32x2 R0[8];
    {
      f32x2 zc = z1c; asm volatile("" : "+v"(zc));
      f32x2 z2 = cmul(z1c, zc);
      f32x2 z2c = z2; asm volatile("" : "+v"(z2c));
      f32x2 z4 = cmul(z2, z2c);
      R0[0] = W; R0[1] = cmul(W, z1c); R0[2] = cmul(W, z2); R0[3] = cmul(R0[1], z2);
#pragma unroll
      for (int b = 0; b < 4; ++b) R0[4 + b] = cmul(R0[b], z4);
    }
    f32x2 A[17];
    {
      A[1] = a128;
      f32x2 a1c = a128; asm volatile("" : "+v"(a1c));
      A[2] = cmul(A[1], a1c); A[3] = cmul(A[2], A[1]); A[4] = cmul(A[3], A[1]);
      A[5] = cmul(A[4], A[1]); A[6] = cmul(A[4], A[2]); A[7] = cmul(A[4], A[3]); A[8] = cmul(A[5], A[3]);
#pragma unroll
      for (int m = 9; m < 16; ++m) A[m] = cmul(A[8], A[m - 8]);
      A[16] = cmul(A[9], A[7]);
    }
    {
      f32x2 t[8];
#pragma unroll
      for (int b = 0; b < 8; ++b) t[b] = cmul(R0[b], A[1]);
#pragma unroll
      for (int b = 0; b < 8; ++b) { WROWT(b, R0[b]); WROWT(8 + b, t[b]); }
    }
    acc[0] = (f32x4){0, 0, 0, 0};
#pragma unroll
    for (int ks = 0; ks < 4; ++ks) { u32x4 xf = *(const u32x4*)(rd + ks * 64); acc[0] = mfma16(xf, yf[ks], acc[0]); }
    CFENCE();
    STAMP(7);
    {
      f32x2 t[16];
#pragma unroll
      for (int hh = 0; hh < 2; ++hh)
#pragma unroll
        for (int b = 0; b < 4; ++b) t[b + 4 * hh] = cmul(R0[2 * b], A[2 + hh]);
#pragma unroll
      for (int hh = 0; hh < 4; ++hh)
#pragma unroll
        for (int b = 0; b < 2; ++b) t[8 + b + 2 * hh] = cmul(R0[4 * b], A[4 + hh]);
#pragma unroll
      for (int r = 0; r < 16; ++r) WROWT(r, t[r]);
    }
    acc[1] = (f32x4){0, 0, 0, 0};
#pragma unroll
    for (int ks = 0; ks < 4; ++ks) { u32x4 xf = *(const u32x4*)(rd + ks * 64); acc[1] = mfma16(xf, yf[ks], acc[1]); }
    CFENCE();
    STAMP(9);
    __builtin_amdgcn_s_setprio(3);
    ua = ld16c(rWS, uvo, WS_XT); ub = ld16c(rWS, uvo + 16, WS_XT); uc = ld16c(rWS, cq < 255 ? uvo + 32 : uvo, WS_XT);
    __builtin_amdgcn_s_setprio(BPRIO);
    __builtin_amdgcn_sched_barrier(0);
    {
      f32x2 t[9];
#pragma unroll
      for (int hh = 0; hh < 9; ++hh) t[hh] = cmul(W, A[8 + hh]);
#pragma unroll
      for (int hh = 0; hh < 9; ++hh) WROWT(hh, t[hh]);
    }
    acc[2] = (f32x4){0, 0, 0, 0};
#pragma unroll
    for (int ks = 0; ks < 4; ++ks) { u32x4 xf = *(const u32x4*)(rd + ks * 64); acc[2] = mfma16(xf, yf[ks], acc[2]); }
    CFENCE();
    float* kt = (float*)slab;
    *(f32x4*)(kt + 8 * l15 + 4 * (g & 1) + 128 * (g >> 1)) = acc[0];
    {
      const int t0 = g < 2 ? 256 + 4 * l15 + 64 * g : 384 + 2 * l15 + 64 * (g - 2);
      const int t1 = g < 2 ? t0 + 2 : t0 + 32;
      *(f32x2*)(kt + t0) = (f32x2){acc[1][0], acc[1][1]};
      *(f32x2*)(kt + t1) = (f32x2){acc[1][2], acc[1][3]};
    }
#pragma unroll
    for (int i = 0; i < 4; ++i) kt[512 + l15 + 16 * (4 * g + i)] = acc[2][i];
  }
  STAMP(2);
  {
    __builtin_amdgcn_s_setprio(3);
#pragma unroll 1
    for (int it = 0; it < MAXSPIN; ++it) {
      const u32 m = (ua[0] ^ tw) | (ua[1] ^ tw) | (ua[2] ^ tw) | (ua[3] ^ tw) | (ub[0] ^ tw) | (ub[1] ^ tw) | (ub[2] ^ tw) | (ub[3] ^ tw) | (uc[0] ^ tw) | (uc[1] ^ tw) | (uc[2] ^ tw);
      if (__all(m < 0x10000u)) break;
      __builtin_amdgcn_s_sleep(2);
      ua = ld16c(rWS, uvo, WS_XT); ub = ld16c(rWS, uvo + 16, WS_XT); uc = ld16c(rWS, cq < 255 ? uvo + 32 : uvo, WS_XT);
    }
    __builtin_amdgcn_s_setprio(BPRIO);
    STAMP(10);
    u32 e[6] = {__builtin_amdgcn_perm(ua[1], ua[0], 0x05040100u), __builtin_amdgcn_perm(ua[3], ua[2], 0x05040100u), __builtin_amdgcn_perm(ub[1], ub[0], 0x05040100u),
                __builtin_amdgcn_perm(ub[3], ub[2], 0x05040100u), __builtin_amdgcn_perm(uc[1], uc[0], 0x05040100u), uc[2] & 0xffffu};
    if (cq == 255) { e[4] = 0; e[5] = 0; }
    char* base = lds + B_UC + cch * UCH + (16 + cq * 8) * 2;
    u32 sh[5];
#pragma unroll
    for (int q = 0; q < 5; ++q) sh[q] = __builtin_amdgcn_alignbit(e[q + 1], e[q], 16);
    *(u32x2*)(base + UC0) = (u32x2){e[0], e[1]}; *(u32x2*)(base + UC0 + 8) = (u32x2){e[2], e[3]};
    *(u32x4*)(base + UC1) = (u32x4){sh[0], sh[1], sh[2], sh[3]};
    *(u32x4*)(base + UC2) = (u32x4){e[1], e[2], e[3], e[4]};
    *(u32x4*)(base + UC3) = (u32x4){sh[1], sh[2], sh[3], sh[4]};
    if (cq == 0) {
      char* b0 = lds + B_UC + cch * UCH;
      const u32x4 z = {0, 0, 0, 0};
      *(u32x2*)(b0 + UC0) = (u32x2){0, 0}; *(u32x2*)(b0 + UC0 + 8) = (u32x2){0, 0}; *(u32x2*)(b0 + UC0 + 16) = (u32x2){0, 0}; *(u32x2*)(b0 + UC0 + 24) = (u32x2){0, 0};
      *(u32x4*)(b0 + UC1) = z; *(u32x4*)(b0 + UC1 + 16) = (u32x4){0, 0, 0, e[0] << 16};
      *(u32x4*)(b0 + UC2) = z; *(u32x4*)(b0 + UC2 + 16) = (u32x4){0, 0, 0, e[0]};
      *(u32x4*)(b0 + UC3) = z; *(u32x4*)(b0 + UC3 + 16) = (u32x4){0, 0, e[0] << 16, sh[0]};
    }
  }
  __builtin_amdgcn_s_setprio(BPRIO2);
  bar_l();
  STAMP(3);
  if (tid < 384) {
    const int ch = tid >= 192 ? 1 : 0, t4 = (tid - 192 * ch) * 4;
    const float* k0 = (const float*)(lds + 4 * ch * KG_SLAB) + t4;
    f32x4 s = *(const f32x4*)k0;
#pragma unroll
    for (int q = 1; q < 4; ++q) s += *(const f32x4*)(k0 + q * (KG_SLAB / 4));
    *(u32x2*)(lds + B_KTAB + ch * 1536 + t4 * 2) = (u32x2){pk2(s[0], s[1]), pk2(s[2], s[3])};
  }
  bar_l();
  {
    const int jhi = 16 * (cq >> 1) + 15 - (cq & 1) * 8;
    const float fj = (float)jhi;
    const char* kt = lds + B_KTAB + cch * 1536;
    u32 o[4];
#pragma unroll
    for (int e = 0; e < 4; ++e) {
      u32 bits = pk2(fj - (float)(2 * e), fj - (float)(2 * e + 1));
      u32 t0 = (bits & 0xffffu) - 0x4280u, t1 = (bits >> 16) - 0x4280u;
      if (jhi < 256) { t0 = jhi - 2 * e; t1 = jhi - 2 * e - 1; }
      u32 lo = *(const u16*)(kt + t0 * 2), hi = *(const u16*)(kt + t1 * 2);
      o[e] = lo | (hi << 16);
    }
    char* kr = lds + B_KR + cch * 4608;
    *(u32x4*)(kr + 512 + cq * 16) = (u32x4){o[0], o[1], o[2], o[3]};
    if (cq < 32) *(u32x4*)(kr + cq * 16) = (u32x4){0, 0, 0, 0};
  }
  bar_l();
  STAMP(4);
  {
    const int ch = w >> 2, rh = (w >> 1) & 1, sh = w & 1;
    const int idx0 = 8 * g + l15 + 1, cc = idx0 & 3;
    const u32 ucb = cc == 0 ? UC0 : (cc == 1 ? UC1 : (cc == 2 ? UC2 : UC3));
    const LAS char* abase = (const LAS char*)(lds + B_UC + ch * UCH + ucb + (idx0 & ~3) * 2 + rh * 256);
    const char* bbase = lds + B_KR + ch * 4608 + (256 + 16 * (l15 - (g >> 1)) + 8 * (g & 1)) * 2 - rh * 256;
    f32x4 c4[4];
#pragma unroll
    for (int i = 0; i < 4; ++i) c4[i] = (f32x4){0, 0, 0, 0};
    if (sh == 0) {
#pragma unroll 1
      for (int rr = 0; rr < 4; ++rr) {
        u32x4 af[8], bf[8];
#pragma unroll
        for (int a = 0; a < 8; ++a) {
          const LAS char* pa = abase + (256 * a) * 2 + rr * 64;
          u32x2 lo = *(const volatile LAS u32x2*)pa, hi = *(const volatile LAS u32x2*)(pa + 8);
          af[a] = (u32x4){lo[0], lo[1], hi[0], hi[1]};
          bf[a] = *(const u32x4*)(bbase + (256 * a) * 2 - rr * 64);
        }
        c4[0] = mfma16(af[0], bf[0], c4[0]);
        c4[1] = mfma16(af[0], bf[1], c4[1]); c4[1] = mfma16(af[1], bf[0], c4[1]);
#pragma unroll
        for (int a = 0; a < 8; ++a) {
          if (a < 7) c4[2] = mfma16(af[a], bf[6 - a], c4[2]);
          c4[3] = mfma16(af[a], bf[7 - a], c4[3]);
        }
      }
    } else {
#pragma unroll 1
      for (int rr = 0; rr < 4; ++rr) {
        u32x4 af[6], bf[6];
#pragma unroll
        for (int a = 0; a < 6; ++a) {
          const LAS char* pa = abase + (256 * a) * 2 + rr * 64;
          u32x2 lo = *(const volatile LAS u32x2*)pa, hi = *(const volatile LAS u32x2*)(pa + 8);
          af[a] = (u32x4){lo[0], lo[1], hi[0], hi[1]};
          bf[a] = *(const u32x4*)(bbase + (256 * a) * 2 - rr * 64);
        }
#pragma unroll
        for (int a = 0; a < 6; ++a) {
          if (a < 3) c4[0] = mfma16(af[a], bf[2 - a], c4[0]);
          if (a < 4) c4[1] = mfma16(af[a], bf[3 - a], c4[1]);
          if (a < 5) c4[2] = mfma16(af[a], bf[4 - a], c4[2]);
          c4[3] = mfma16(af[a], bf[5 - a], c4[3]);
        }
      }
    }
    STAMP(5);
    const f32x4 e0 = c4[0], e1 = c4[1], e2 = c4[2], e3 = c4[3];
    f32x4* xw = (f32x4*)(lds + B_RED + w * 2048) + lane;
    xw[0] = rh ? e0 : e2; xw[64] = rh ? e1 : e3;
    bar_l();
    const f32x4* xr = (const f32x4*)(lds + B_RED + (w ^ 2) * 2048) + lane;
    f32x4 v0 = xr[0], v1 = xr[64];
    v0 += rh ? e2 : e0; v1 += rh ? e3 : e1;
    const int pt = sh == 0 ? (rh ? 6 : 0) : (rh ? 4 : 2);
    auto s0 = __builtin_amdgcn_permlane16_swap(pk2(v0[0], v0[1]), pk2(v1[0], v1[1]), false, false);
    auto s1 = __builtin_amdgcn_permlane16_swap(pk2(v0[2], v0[3]), pk2(v1[2], v1[3]), false, false);
    __builtin_amdgcn_s_setprio(3);
    const u32 yo = (ch0 + ch) * (S * 4) + (256 * (pt + (g & 1)) + 16 * l15 + 8 * (g >> 1)) * 4;
    st16c((u32x4){(s0[0] & 0xffffu) | tw, (s0[0] >> 16) | tw, (s1[0] & 0xffffu) | tw, (s1[0] >> 16) | tw}, rWS, yo, WS_YT);
    st16c((u32x4){(s0[1] & 0xffffu) | tw, (s0[1] >> 16) | tw, (s1[1] & 0xffffu) | tw, (s1[1] >> 16) | tw}, rWS, yo + 16, WS_YT);
    STAMP(6);
  }
  STAMP_FLUSH(ws);
}

__global__ __launch_bounds__(512, 4) void fused(Params p) {
  __shared__ __attribute__((aligned(16))) char lds[LDS_BYTES];
  const int b = blockIdx.x, x8 = b & 7, j = b >> 3;
  const unsigned long long t0_ = __builtin_amdgcn_s_memtime();
  if (j < 32) roleA(p, x8, j, lds);
  else roleB(p, x8, j - 32, lds);
  if (b == 0) { while (__builtin_amdgcn_s_memtime() - t0_ < 84000) __builtin_amdgcn_s_sleep(4); }
}

extern "C" void kernel_launch(void* const* d_in, const int* in_sizes, int n_in, void* d_out, int out_size, void* d_ws, size_t ws_size, hipStream_t stream) {
  Params p;
  for (int i = 0; i < 14; ++i) p.in[i] = (const u16*)d_in[i];
  p.ws = (char*)d_ws; p.out = (u16*)d_out;
  fused<<<480, 512, 0, stream>>>(p);
}
```

```cpp
#include <hip/hip_runtime.h>
#include <stdint.h>

typedef unsigned short u16;
typedef unsigned int u32;
typedef short s16x4 __attribute__((ext_vector_type(4)));
typedef __bf16 bf16x8 __attribute__((ext_vector_type(8)));
typedef __bf16 bf16x2 __attribute__((ext_vector_type(2)));
typedef float f32x4 __attribute__((ext_vector_type(4)));
typedef float f32x2 __attribute__((ext_vector_type(2)));
typedef u32 u32x4 __attribute__((ext_vector_type(4)));
typedef u32 u32x2 __attribute__((ext_vector_type(2)));
typedef __amdgpu_buffer_rsrc_t rsrc_t;
#define LAS __attribute__((address_space(3)))
#define GAS __attribute__((address_space(1)))
#define DI __device__ __forceinline__

constexpr int S = 2048, D = 448, H = 16, DH = 28, NM = 256;

constexpr u32 WS_GEN = 0;
constexpr u32 WS_FLAG = 4096, FSTR = 128;
constexpr u32 F_B = 0;
constexpr u32 F_A = 224;
constexpr u32 NFLAG = F_A + 256;
constexpr u32 WS_XT = 1u << 20;
constexpr u32 WS_KVP = WS_XT + D * S * 4;
constexpr u32 WS_OF = WS_KVP + 16 * 16 * 4 * 1024;
constexpr u32 WS_YT = WS_OF + 128 * 16 * 1024;
constexpr u32 WS_GATE = WS_YT + D * S * 4;
constexpr u32 WS_END = WS_GATE + S * 4;
static_assert(WS_FLAG + NFLAG * FSTR <= WS_XT, "flags");
constexpr int MAXSPIN = 400000;
#define CODE_BYTES 8192

#define STAMP(i)
#define STAMP_DECL
#define STAMP_FLUSH(ws)

DI float bflo(u32 v) { return __uint_as_float(v << 16); }
DI float bfhi(u32 v) { return __uint_as_float(v & 0xffff0000u); }
DI u32 pk2(float lo, float hi) { f32x2 p = {lo, hi}; bf16x2 q = __builtin_convertvector(p, bf16x2); return __builtin_bit_cast(u32, q); }
DI float smul(float a, float b) { float r; asm("v_mul_f32 %0, %1, %2" : "=v"(r) : "v"(a), "v"(b)); return r; }
DI float rbf(float f) { return bflo(pk2(f, f)); }
DI f32x4 mfma16(u32x4 a, u32x4 b, f32x4 c) {
  return __builtin_amdgcn_mfma_f32_16x16x32_bf16(__builtin_bit_cast(bf16x8, a), __builtin_bit_cast(bf16x8, b), c, 0, 0, 0);
}
DI u32x2 trread(const char* lds_base, u32 off) {
  s16x4 t = __builtin_amdgcn_ds_read_tr16_b64_v4i16((LAS s16x4*)((LAS char*)lds_base + off));
  return __builtin_bit_cast(u32x2, t);
}
DI rsrc_t mkrs(const void* p, u32 bytes) { return __builtin_amdgcn_make_buffer_rsrc((void*)p, 0, bytes, 0x00020000); }
DI void dma16(rsrc_t r, char* lds_dst, u32 voff, u32 soff) { __builtin_amdgcn_raw_ptr_buffer_load_lds(r, (LAS void*)lds_dst, 16, voff, soff, 0, 0); }
DI void touch4(rsrc_t r, char* lds_dst, u32 voff, u32 soff) { __builtin_amdgcn_raw_ptr_buffer_load_lds(r, (LAS void*)lds_dst, 4, voff, soff, 0, 0); }
DI u32x4 mkrs4(const void* p, u32 bytes) { unsigned long long a = (unsigned long long)p; return (u32x4){(u32)a, (u32)(a >> 32) & 0xffffu, bytes, 0x00020000u}; }
DI u32 ldsaddr(const char* p) { return (u32)(uintptr_t)(LAS const char*)p; }
DI void adma16(u32x4 rs, u32 lds_dst, u32 voff, u32 soff) {
  asm volatile("s_mov_b32 m0, %0\n\ts_nop 0\n\tbuffer_load_dwordx4 %1, %2, %3 offen lds" ::"s"(lds_dst), "v"(voff), "s"(rs), "s"(soff) : "memory");
}
DI void adma16h(u32x4 rs, u32 lds_dst, u32 voff, u32 soff) {
  asm volatile("s_mov_b32 m0, %0\n\ts_mov_b32 exec_hi, 0\n\tbuffer_load_dwordx4 %1, %2, %3 offen lds\n\ts_mov_b32 exec_hi, -1" ::"s"(lds_dst), "v"(voff), "s"(rs), "s"(soff) : "memory");
}
DI u32 sload(const void* p) { return *(const __attribute__((address_space(4))) u32*)(unsigned long long)p; }
DI void atouch4(u32x4 rs, u32 lds_dst, u32 voff, u32 soff) {
  asm volatile("s_mov_b32 m0, %0\n\ts_nop 0\n\tbuffer_load_dword %1, %2, %3 offen lds" ::"s"(lds_dst), "v"(voff), "s"(rs), "s"(soff) : "memory");
}
DI u32x4 ald16(u32x4 rs, u32 voff, u32 soff) {
  u32x4 r; asm volatile("buffer_load_dwordx4 %0, %1, %2, %3 offen" : "=v"(r) : "v"(voff), "s"(rs), "s"(soff) : "memory"); return r;
}
template <int N> DI void waitv4(u32x4& a, u32x4& b, u32x4& c, u32x4& d) { asm volatile("s_waitcnt vmcnt(%4)" : "+v"(a), "+v"(b), "+v"(c), "+v"(d) : "i"(N) : "memory"); }
DI u32x4 ld16(rsrc_t r, u32 voff, u32 soff) { return __builtin_amdgcn_raw_buffer_load_b128(r, voff, soff, 0); }
DI u32x4 ld16c(rsrc_t r, u32 voff, u32 soff) { return __builtin_amdgcn_raw_buffer_load_b128(r, voff, soff, 16); }
DI u32x2 ld8c(rsrc_t r, u32 voff, u32 soff) { return __builtin_amdgcn_raw_buffer_load_b64(r, voff, soff, 16); }
DI u32 ld4c(rsrc_t r, u32 voff, u32 soff) { return __builtin_amdgcn_raw_buffer_load_b32(r, voff, soff, 16); }
DI u32 ld2c(rsrc_t r, u32 voff, u32 soff) { return __builtin_amdgcn_raw_buffer_load_b16(r, voff, soff, 16); }
DI void st16c(u32x4 v, rsrc_t r, u32 voff, u32 soff) { __builtin_amdgcn_raw_buffer_store_b128(v, r, voff, soff, 16); }
DI void st4c(u32 v, rsrc_t r, u32 voff, u32 soff) { __builtin_amdgcn_raw_buffer_store_b32(v, r, voff, soff, 16); }
#define WAIT_L(n) asm volatile("s_waitcnt lgkmcnt(" #n ")" ::: "memory")
#define BAR() __builtin_amdgcn_s_barrier()
#define CFENCE() asm volatile("" ::: "memory")
template <int N> DI void waitv() { asm volatile("s_waitcnt vmcnt(%0)" ::"i"(N) : "memory"); }
DI void bar_l() { WAIT_L(0); BAR(); CFENCE(); }

DI u32 flag_off(u32 idx) { return WS_FLAG + idx * FSTR; }
DI void publish(char* ws, u32 idx, u32 tag, int lane) {
  waitv<0>();
  if (lane == 0) __hip_atomic_store((u32*)(ws + flag_off(idx)), tag, __ATOMIC_RELAXED, __HIP_MEMORY_SCOPE_AGENT);
}
DI void pollf(char* ws, u32 idx, u32 tag) {
  const u32* f = (const u32*)(ws + flag_off(idx));
#pragma unroll 1
  for (int it = 0; it < MAXSPIN; ++it) {
    u32 v = __hip_atomic_load(f, __ATOMIC_RELAXED, __HIP_MEMORY_SCOPE_AGENT);
    if (__all(v == tag)) break;
    __builtin_amdgcn_s_sleep(1);
  }
  CFENCE();
}

DI float red_max_g(float x) {
  u32 u = __float_as_uint(x);
  auto r = __builtin_amdgcn_permlane16_swap(u, u, false, false);
  x = fmaxf(__uint_as_float(r[0]), __uint_as_float(r[1]));
  u = __float_as_uint(x);
  auto r2 = __builtin_amdgcn_permlane32_swap(u, u, false, false);
  return fmaxf(__uint_as_float(r2[0]), __uint_as_float(r2[1]));
}
DI float red_sum_g(float x) {
  u32 u = __float_as_uint(x);
  auto r = __builtin_amdgcn_permlane16_swap(u, u, false, false);
  x = __uint_as_float(r[0]) + __uint_as_float(r[1]);
  u = __float_as_uint(x);
  auto r2 = __builtin_amdgcn_permlane32_swap(u, u, false, false);
  return __uint_as_float(r2[0]) + __uint_as_float(r2[1]);
}
template <int CTRL> DI float dpp_add(float x) {
  return x + __uint_as_float(__builtin_amdgcn_update_dpp(0, __float_as_uint(x), CTRL, 0xf, 0xf, false));
}
DI float red_sum_all(float x) {
  x = dpp_add<0x128>(x); x = dpp_add<0x124>(x); x = dpp_add<0x122>(x); x = dpp_add<0x121>(x);
  return red_sum_g(x);
}

#define LDECL
#define LST(k)
#define LST2(k)
#define LFLUSH(ws)
#define P2SLEEP 2
#define DBGCNT(x)
#define LD1 ld16
#define LD2 ld16
#define BPRIO2 0
#define APRIO2 2
#define APRIO 0
#define BPRIO 2
DI void dual_softmax(const f32x4 (&pacc)[2], u32x4& pos, u32x4& neg) {
  constexpr float L2E = 1.4426950408889634f;
  float p[7];
  {
    u32 a = pk2(pacc[0][0], pacc[0][1]), b = pk2(pacc[0][2], pacc[0][3]), c = pk2(pacc[1][0], pacc[1][1]), d = pk2(pacc[1][2], pacc[1][2]);
    p[0] = bflo(a); p[1] = bfhi(a); p[2] = bflo(b); p[3] = bfhi(b); p[4] = bflo(c); p[5] = bfhi(c); p[6] = bflo(d);
  }
  float mx = fmaxf(fmaxf(fmaxf(p[0], p[1]), fmaxf(p[2], p[3])), fmaxf(fmaxf(p[4], p[5]), p[6]));
  float mn = fminf(fminf(fminf(p[0], p[1]), fminf(p[2], p[3])), fminf(fminf(p[4], p[5]), p[6]));
  mx = red_max_g(mx);
  mn = -red_max_g(-mn);
  float ep[7], en[7];
  float sp = 0.f, sn = 0.f;
  const float mxl = mx * L2E, mnl = mn * L2E;
#pragma unroll
  for (int i = 0; i < 7; ++i) {
    ep[i] = __builtin_amdgcn_exp2f(__builtin_fmaf(p[i], L2E, -mxl));
    en[i] = __builtin_amdgcn_exp2f(__builtin_fmaf(p[i], -L2E, mnl));
    sp += ep[i]; sn += en[i];
  }
  sp = red_sum_g(sp); sn = red_sum_g(sn);
  const float rp = __builtin_amdgcn_rcpf(sp), rn = __builtin_amdgcn_rcpf(sn);
#pragma unroll
  for (int i = 0; i < 3; ++i) {
    pos[i] = pk2(ep[2 * i] * rp, ep[2 * i + 1] * rp);
    neg[i] = pk2(en[2 * i] * rn, en[2 * i + 1] * rn);
  }
  pos[3] = pk2(ep[6] * rp, 0.f); neg[3] = pk2(en[6] * rn, 0.f);
}
DI float acc_sel(u32 v, u32 sel, float a) { return __builtin_amdgcn_fdot2_f32_bf16(__builtin_bit_cast(bf16x2, v), __builtin_bit_cast(bf16x2, sel), a, false); }
DI bool tag_ok(u32 v3, u32 t16) { return __all((v3 >> 16) == t16); }

struct Params { const u16* in[14]; char* ws; u16* out; };

constexpr int A_X = 0, A_W = 32768, A_WPHI = 73728, A_GW = 75776, A_VT = 16384, A_Q6 = 24576, A_KVB = 76800, A_RED = 65536;
constexpr int LDS_BYTES = 81920;
constexpr int WROW = 3 * D * 2;
constexpr int WCH = 64 * WROW;

DI void roleA(const Params& p, int x8, int j, char* lds) {
  const int tid = threadIdx.x, lane = tid & 63, w = __builtin_amdgcn_readfirstlane(tid >> 6);
  const int g = lane >> 4, l15 = lane & 15, q4 = l15 >> 2, pp = lane & 3;
  const int h = 4 * (x8 & 3) + (j & 3), tile = 8 * (x8 >> 2) + (j >> 2);
  const int rot = (2 * (h & 3) + (tile & 7)) % 7;
  char* ws = p.ws;
  LDECL
  STAMP_DECL
  STAMP(0);
  __builtin_amdgcn_s_setprio(3);
  const u32x4 rX = mkrs4(p.in[0], S * D * 2), rW = mkrs4(p.in[1], D * 3 * D * 2);
  const rsrc_t rWS = mkrs(ws, WS_END);
  const u32 lb = ldsaddr(lds);
  const u32 gen = sload(ws + WS_GEN);
  const int tok0 = tile * 128 + w * 16;
  u32 xvo[2];
#pragma unroll
  for (int i = 0; i < 2; ++i) { int row = 8 * i + (lane >> 3); xvo[i] = (tok0 + row) * (D * 2) + (((lane & 7) ^ ((row >> 1) & 7)) * 16); }
  u32 wvo;
  { int row = 16 * (w & 3) + (lane >> 2); int cs = (lane & 3) ^ (2 * ((row >> 3) & 1)); wvo = row * WROW + h * (DH * 2) + (cs < 3 ? cs * 16 : 40); }
  auto cidx = [&](int i) { int c = rot + i; return c >= 7 ? c - 7 : c; };
  auto issueX = [&](int i) {
    const u32 so = cidx(i) * 128, dst = lb + A_X + (i & 1) * 16384 + w * 2048;
    adma16(rX, dst, xvo[0], so); adma16(rX, dst + 1024, xvo[1], so);
  };
  auto issueW = [&](int v) {
    const u32 dst = lb + A_W + (v % 5) * 8192 + w * 1024;
    if (v < 7) adma16(rW, dst, wvo, cidx(v) * WCH + (1 + (w >> 2)) * (D * 2));
    else adma16(rW, dst, wvo, cidx(2 * (v - 7) + (w >> 2)) * WCH);
  };
  if (w < 2) adma16(mkrs4(p.in[2], H * DH * DH * 2), lb + A_WPHI + w * 1024, lane * 16, h * (DH * DH * 2) + w * 1024);
  else if (w == 2) { int c = rot + (lane >> 3); c = c >= 7 ? c - 7 : c; adma16(mkrs4(p.in[13], D * 2), lb + A_GW, lane < 56 ? c * 128 + (lane & 7) * 16 : 4096, 0); }
  issueW(0); issueX(0);
  __builtin_amdgcn_s_setprio(2);
  issueW(1); issueX(1);
  __builtin_amdgcn_s_setprio(1);
  issueW(2);
  __builtin_amdgcn_s_setprio(APRIO);
  __builtin_amdgcn_sched_barrier(0);
  STAMP(1);
  f32x4 ak[2] = {{0, 0, 0, 0}, {0, 0, 0, 0}}, av[2] = {{0, 0, 0, 0}, {0, 0, 0, 0}};
  u32 xoff[2];
#pragma unroll
  for (int ks = 0; ks < 2; ++ks) xoff[ks] = w * 2048 + l15 * 128 + (((4 * ks + g) ^ ((l15 >> 1) & 7)) * 16);
  u32 woff[2];
#pragma unroll
  for (int ct = 0; ct < 2; ++ct) woff[ct] = (8 * g + q4) * 64 + ((ct ^ (g & 1)) * 32) + pp * 8;
  u32x4 xf[14];
  u32x4 wf0[2][2], wf1[2][2];
  auto readX = [&](int i) {
    const char* xs = lds + A_X + (i & 1) * 16384;
    xf[2 * i] = *(const u32x4*)(xs + xoff[0]); xf[2 * i + 1] = *(const u32x4*)(xs + xoff[1]);
  };
  auto readW = [&](int i, int ks, u32x4 (&wf)[2][2]) {
    const char* wsb = lds + A_W + (i % 5) * 8192;
#pragma unroll
    for (int r = 0; r < 2; ++r)
#pragma unroll
      for (int ct = 0; ct < 2; ++ct) {
        u32x2 a = trread(wsb, woff[ct] + r * 4096 + ks * 2048);
        u32x2 b = trread(wsb, woff[ct] + r * 4096 + ks * 2048 + 256);
        wf[r][ct] = (u32x4){a[0], a[1], b[0], b[1]};
      }
  };
  auto mm = [&](const u32x4& x, const u32x4 (&wf)[2][2]) {
#pragma unroll
    for (int ct = 0; ct < 2; ++ct) { ak[ct] = mfma16(wf[0][ct], x, ak[ct]); av[ct] = mfma16(x, wf[1][ct], av[ct]); }
  };
  waitv<3>();
  BAR();
  CFENCE();
  readX(0); readW(0, 0, wf0);
  __builtin_amdgcn_sched_barrier(0);
  readW(0, 1, wf1);
  __builtin_amdgcn_sched_barrier(0);
  WAIT_L(8);
  issueX(2); issueW(3);
#pragma unroll
  for (int i = 0; i < 7; ++i) {
    __builtin_amdgcn_sched_barrier(0);
    mm(xf[2 * i], wf0);
    __builtin_amdgcn_sched_barrier(0);
    if (i < 6) {
      if (i == 0 || i == 3 || i == 4) waitv<4>(); else if (i == 5) { if (w < 4) waitv<3>(); else waitv<2>(); } else waitv<5>();
      LST(2 * i);
      readX(i + 1); readW(i + 1, 0, wf0);
    }
    __builtin_amdgcn_sched_barrier(0);
    mm(xf[2 * i + 1], wf1);
    __builtin_amdgcn_sched_barrier(0);
    if (i == 1 && w == 7) {
      u32x4 wph[2];
#pragma unroll
      for (int ft = 0; ft < 2; ++ft) {
        int f = 7 * q4 + 4 * ft + pp; f = f > 27 ? 27 : f;
        const char* wp = lds + A_WPHI + f * 2;
        const int d0 = 4 * g, d1 = g < 2 ? 16 + 4 * g : 24;
        u32 e[8];
#pragma unroll
        for (int jj = 0; jj < 4; ++jj) { e[jj] = *(const u16*)(wp + (d0 + jj) * 56); e[4 + jj] = *(const u16*)(wp + (d1 + jj) * 56); }
        wph[ft] = (u32x4){e[0] | (e[1] << 16), e[2] | (e[3] << 16), g == 2 ? 0u : (e[4] | (e[5] << 16)), g == 2 ? 0u : (e[6] | (e[7] << 16))};
      }
      CFENCE();
      *(u32x4*)(lds + A_WPHI + lane * 16) = wph[0]; *(u32x4*)(lds + A_WPHI + 1024 + lane * 16) = wph[1];
      WAIT_L(0);
    }
    if (i < 6) {
      readW(i + 1, 1, wf1);
      __builtin_amdgcn_sched_barrier(0);
      WAIT_L(8);
      if (i + 3 < 7) issueX(i + 3);
      if (i == 0) issueW(4);
      if (i < 5) {
        if (i < 3) waitv<6>();
        LST(2 * i + 1);
        BAR();
        CFENCE();
        LST2(i);
        issueW(i + 5);
        if (i == 4 && w < 4) adma16(rW, lb + A_Q6 + w * 1024, wvo, cidx(6) * WCH);
      }
    }
  }
  __builtin_amdgcn_s_setprio(APRIO2);
  STAMP(2);
  const u32 tag = gen + 1u, t16 = tag & 0xffffu;
  char* phk = lds + A_X; char* vt = lds + A_VT;
  {
    const int tl = w * 16 + l15;
    u32x4 kb = {pk2(ak[0][0], ak[0][1]), pk2(ak[0][2], ak[0][3]), pk2(ak[1][0], ak[1][1]), pk2(ak[1][2], ak[1][3])};
    f32x4 z4 = {0, 0, 0, 0}; f32x4 pk[2];
    pk[0] = mfma16(*(const u32x4*)(lds + A_WPHI + lane * 16), kb, z4); pk[1] = mfma16(*(const u32x4*)(lds + A_WPHI + 1024 + lane * 16), kb, z4);
    u32x4 pos, neg; dual_softmax(pk, pos, neg);
    const int sw = ((tl >> 1) & 1) | (((tl >> 3) & 1) << 1);
    char* rowp = phk + tl * 128 + g * 8;
    *(u32x2*)(rowp + ((0 ^ sw) * 32)) = (u32x2){pos[0], pos[1]};
    *(u32x2*)(rowp + ((1 ^ sw) * 32)) = (u32x2){pos[2], pos[3]};
    *(u32x2*)(rowp + ((2 ^ sw) * 32)) = (u32x2){neg[0], neg[1]};
    *(u32x2*)(rowp + ((3 ^ sw) * 32)) = (u32x2){neg[2], neg[3]};
  }
  STAMP(3);
  {
#pragma unroll
    for (int ct = 0; ct < 2; ++ct) {
      int d = ct == 0 ? l15 : (l15 < 8 ? 16 + l15 : 12 + l15);
      int G = (d * 37) >> 8, e = d - 7 * G;
      int rho = e < 4 ? 4 * G + e : 12 + 4 * G + e;
      f32x4 v = av[ct];
      if (ct == 1 && (l15 >> 2) == 2) { rho = pp == 0 ? 27 : (pp == 1 ? 19 : (pp == 2 ? 23 : 31)); if (pp == 0) v = (f32x4){1.f, 1.f, 1.f, 1.f}; }
      *(u32x2*)(vt + rho * 256 + (((2 * w + (g >> 1)) ^ (rho & 15)) * 16) + (g & 1) * 8) = (u32x2){pk2(v[0], v[1]), pk2(v[2], v[3])};
    }
  }
  bar_l();
  STAMP(4);
  if (w < 4) {
    const int sg = w >> 1, dt = w & 1;
    f32x4 t0 = {0, 0, 0, 0}, t1 = t0;
#pragma unroll
    for (int ks = 0; ks < 4; ++ks) {
      u32x4 af[2];
#pragma unroll
      for (int ft = 0; ft < 2; ++ft) {
        u32x2 a[2];
#pragma unroll
        for (int t = 0; t < 2; ++t) {
          int row = 32 * ks + 8 * g + 4 * t + q4;
          int sw = ((row >> 1) & 1) | (((row >> 3) & 1) << 1);
          a[t] = trread(phk, row * 128 + (((2 * sg + ft) ^ sw) * 32) + pp * 8);
        }
        af[ft] = (u32x4){a[0][0], a[0][1], a[1][0], a[1][1]};
      }
      u32x4 bf = *(const u32x4*)(vt + (16 * dt + l15) * 256 + (((4 * ks + g) ^ l15) * 16));
      t0 = mfma16(af[0], bf, t0); t1 = mfma16(af[1], bf, t1);
    }
    u32x4 un = {pk2(t0[0], t0[1]), pk2(t0[2], t0[3]), pk2(t1[0], t1[1]), (pk2(t1[2], t1[2]) & 0xffffu) | (t16 << 16)};
    st16c(un, rWS, (((h * 16 + tile) * 4 + w) * 64 + lane) * 16, WS_KVP);
    waitv<1>();
  } else waitv<0>();
  STAMP(5);
  BAR();
  CFENCE();
  u32x4 pqp, pqn;
  u32x4 pv[16];
  const u32 pvo = ((h * 16 * 4 + (w & 3)) * 64 + lane) * 16;
  const u32 plo = WS_KVP + (((h * 16 + l15) * 4 + (w & 3)) * 64 + 63) * 16 + 12;
  u32 pl = 0;
  bool got = false;
  if (w < 4) pl = ld4c(rWS, plo, 0);
  auto loadPV = [&]() {
#pragma unroll
    for (int t = 0; t < 16; ++t) pv[t] = LD1(rWS, pvo, WS_KVP + ((t + (tile & 3) * 4) & 15) * 4096);
  };
  {
    f32x4 aq[2] = {{0, 0, 0, 0}, {0, 0, 0, 0}};
#pragma unroll
    for (int i = 0; i < 7; ++i) {
      const char* qb = lds + (i == 6 ? A_Q6 : A_W + ((7 + (i >> 1)) % 5) * 8192 + (i & 1) * 4096);
#pragma unroll
      for (int ks = 0; ks < 2; ++ks)
#pragma unroll
        for (int ct = 0; ct < 2; ++ct) {
          u32x2 a = trread(qb, woff[ct] + ks * 2048);
          u32x2 b = trread(qb, woff[ct] + ks * 2048 + 256);
          aq[ct] = mfma16((u32x4){a[0], a[1], b[0], b[1]}, xf[2 * i + ks], aq[ct]);
        }
    }
    const u32x4 wq0 = *(const u32x4*)(lds + A_WPHI + lane * 16), wq1 = *(const u32x4*)(lds + A_WPHI + 1024 + lane * 16);
    if (2 * w == h || 2 * w + 1 == h) {
      if (((h ^ tile) & 1) == 0) {
        f32x4 ga = {0, 0, 0, 0};
#pragma unroll
        for (int i = 0; i < 14; ++i) ga = mfma16(*(const u32x4*)(lds + A_GW + i * 64 + g * 16), xf[i], ga);
        const float gate = rbf(__builtin_amdgcn_rcpf(1.f + __expf(-rbf(ga[0]))));
        if (lane < 16) st4c((__float_as_uint(gate) >> 16) | (t16 << 16), rWS, (tok0 + lane) * 4, WS_GATE);
      }
    }
    bar_l();
    if (w < 4) {
      got = __all((pl >> 16) == t16);
      if (got) loadPV(); else pl = ld4c(rWS, plo, 0);
    }
    __builtin_amdgcn_sched_barrier(0);
    u32x4 qb = {pk2(aq[0][0], aq[0][1]), pk2(aq[0][2], aq[0][3]), pk2(aq[1][0], aq[1][1]), pk2(aq[1][2], aq[1][3])};
    f32x4 z4 = {0, 0, 0, 0}; f32x4 pq[2];
    pq[0] = mfma16(wq0, qb, z4); pq[1] = mfma16(wq1, qb, z4);
    dual_softmax(pq, pqp, pqn);
    __builtin_amdgcn_sched_barrier(0);
    const bool fin0 = j < 28;
    if (w >= 4 && fin0) {
      const u32x4 rWO = mkrs4(p.in[3], D * D * 2);
      const int jr = lane >> 3;
      const u32 vo = (jr < 7 ? jr : 6) * (D * 2) + (j % 7) * 128 + (((lane & 7) ^ (2 * (((jr >> 1) & 1) | ((w & 1) << 1)))) * 16);
#pragma unroll
      for (int m = 0; m < 16; ++m) adma16(rWO, lb + ((w - 4) + 4 * m) * 1024, vo, ((w - 4) + 4 * m) * (7 * D * 2));
    }
  }
  STAMP(6);
  const bool fin = j < 28;
  const int tt = 4 * x8 + j / 7, cg = j % 7, c0 = cg * 64;
  const int rt = w >> 1, kh = w & 1;
  const int ftok = tt * 64 + rt * 16 + l15;
  if (w < 4) {
    if (!got) {
#pragma unroll 1
      for (int it = 0; it < MAXSPIN; ++it) {
        if (__all((pl >> 16) == t16)) break;
        __builtin_amdgcn_s_sleep(1);
        pl = ld4c(rWS, plo, 0);
      }
      loadPV();
    }
    u32 pend = 15;
#pragma unroll 1
    for (int it = 0; it < MAXSPIN; ++it) {
#pragma unroll
      for (int gq = 0; gq < 4; ++gq)
        if (pend & (1u << gq)) {
          const u32 m = (pv[4 * gq][3] ^ (t16 << 16)) | (pv[4 * gq + 1][3] ^ (t16 << 16)) | (pv[4 * gq + 2][3] ^ (t16 << 16)) | (pv[4 * gq + 3][3] ^ (t16 << 16));
          if (__all(m < 0x10000u)) pend &= ~(1u << gq);
        }
      if (!pend) break;
      __builtin_amdgcn_s_sleep(2);
#pragma unroll
      for (int gq = 0; gq < 4; ++gq)
        if (pend & (1u << gq)) {
#pragma unroll
          for (int t = 0; t < 4; ++t) pv[4 * gq + t] = ld16c(rWS, pvo, WS_KVP + ((4 * gq + t + (tile & 3) * 4) & 15) * 4096);
        }
    }
    float s[7] = {0, 0, 0, 0, 0, 0, 0};
    u32 selo = 0x00003f80u, sehi = 0x3f800000u;
    asm volatile("" : "+s"(selo), "+s"(sehi));
#pragma unroll
    for (int t = 0; t < 16; ++t) {
      s[0] = acc_sel(pv[t][0], selo, s[0]); s[1] = acc_sel(pv[t][0], sehi, s[1]); s[2] = acc_sel(pv[t][1], selo, s[2]); s[3] = acc_sel(pv[t][1], sehi, s[3]);
      s[4] = acc_sel(pv[t][2], selo, s[4]); s[5] = acc_sel(pv[t][2], sehi, s[5]); s[6] += bflo(pv[t][3]);
    }
    *(u32x4*)(lds + A_KVB + w * 1024 + lane * 16) = (u32x4){pk2(s[0], s[1]), pk2(s[2], s[3]), pk2(s[4], s[5]), pk2(s[6], 0.f)};
  }
  bar_l();
  STAMP(7);
  {
    f32x4 o[2];
#pragma unroll
    for (int dt = 0; dt < 2; ++dt) {
      const u32x4 kp = *(const u32x4*)(lds + A_KVB + dt * 1024 + lane * 16), kn = *(const u32x4*)(lds + A_KVB + (2 + dt) * 1024 + lane * 16);
      o[dt] = mfma16(kp, pqp, (f32x4){0, 0, 0, 0});
      o[dt] = mfma16(kn, pqn, o[dt]);
    }
    u32 du = __float_as_uint(o[1][3]);
    auto r1 = __builtin_amdgcn_permlane32_swap(du, du, false, false);
    u32 d1 = r1[1];
    auto r2 = __builtin_amdgcn_permlane16_swap(d1, d1, false, false);
    float den = __uint_as_float(r2[0]);
    den = rbf(rbf(den) + 1e-6f);
    const float rd = __builtin_amdgcn_rcpf(den);
    const u32 n0 = pk2(o[0][0], o[0][1]), n1 = pk2(o[0][2], o[0][3]), n2 = pk2(o[1][0], o[1][1]), n3 = pk2(o[1][2], o[1][2]);
    u32x4 of = {pk2(bflo(n0) * rd, bfhi(n0) * rd), pk2(bflo(n1) * rd, bfhi(n1) * rd), pk2(bflo(n2) * rd, bfhi(n2) * rd), (pk2(bflo(n3) * rd, 0.f) & 0xffffu) | (t16 << 16)};
    const int rt16 = tile * 8 + w;
    st16c(of, rWS, ((rt16 * 16 + h) * 64 + lane) * 16, WS_OF);
  }
  STAMP(8);
  if (fin) {
    const int rt16 = tt * 4 + rt;
    u32x4 of[8];
    u32 yv[2][4], gv;
    const u32 ovo = ((rt16 * 16 + 8 * kh) * 64 + lane) * 16;
    const u32 yvo = (c0 + 32 * kh + 4 * g) * (S * 4) + ftok * 4;
    const u32 tw = t16 << 16;
    {
      const u32 po = lane < 8 ? WS_OF + ((rt16 * 16 + 8 * kh + lane) * 64 + 63) * 16 + 12 : WS_GATE + (rt16 * 16 + 15) * 4;
#pragma unroll 1
      for (int it = 0; it < MAXSPIN; ++it) {
        const u32 v = ld4c(rWS, po, 0);
        if (__all((v >> 16) == t16)) break;
        __builtin_amdgcn_s_sleep(P2SLEEP);
      }
    }
    DBGCNT(st_[12] = (u32)__builtin_amdgcn_s_memrealtime();)
#pragma unroll
    for (int t = 0; t < 8; ++t) of[t] = LD2(rWS, ovo, WS_OF + t * 1024);
    gv = ld4c(rWS, ftok * 4, WS_GATE);
    auto loadY = [&]() {
#pragma unroll
      for (int m = 0; m < 2; ++m)
#pragma unroll
        for (int i = 0; i < 4; ++i) yv[m][i] = ld4c(rWS, yvo, WS_YT + (16 * m + i) * (S * 4));
    };
    loadY();
#pragma unroll 1
    for (int it = 0; it < MAXSPIN; ++it) {
      u32 m = gv ^ tw;
#pragma unroll
      for (int t = 0; t < 8; ++t) m |= of[t][3] ^ tw;
      if (__all(m < 0x10000u)) break;
      __builtin_amdgcn_s_sleep(2);
#pragma unroll
      for (int t = 0; t < 8; ++t) of[t] = ld16c(rWS, ovo, WS_OF + t * 1024);
      gv = ld4c(rWS, ftok * 4, WS_GATE);
    }
    STAMP(9);
    CFENCE();
    BAR();
    CFENCE();
    u32 wa[4];
    { const int s = 2 * (((q4 >> 1) & 1) | ((g & 1) << 1));
#pragma unroll
      for (int ct = 0; ct < 4; ++ct) wa[ct] = kh * 32768 + (8 * g + q4) * 128 + (((2 * ct) ^ s) * 16) + pp * 8; }
    f32x4 acc[4];
#pragma unroll
    for (int ct = 0; ct < 4; ++ct) acc[ct] = (f32x4){0, 0, 0, 0};
#pragma unroll
    for (int hh = 0; hh < 8; ++hh) {
      u32x4 ob = of[hh]; ob[3] &= 0xffffu;
#pragma unroll
      for (int ct = 0; ct < 4; ++ct) {
        u32x2 a = trread(lds, wa[ct] + hh * 4096);
        u32x2 b = trread(lds, wa[ct] + hh * 4096 + 512);
        acc[ct] = mfma16((u32x4){a[0], a[1], b[0], b[1]}, ob, acc[ct]);
      }
    }
    STAMP(10);
    const f32x4 c0_ = acc[0], c1_ = acc[1], c2_ = acc[2], c3_ = acc[3];
    {
      f32x4* rw = (f32x4*)(lds + A_RED + w * 2048) + lane;
      rw[0] = kh ? c0_ : c2_; rw[64] = kh ? c1_ : c3_;
    }
    const f32x4 keep0 = kh ? c2_ : c0_, keep1 = kh ? c3_ : c1_;
    bar_l();
#pragma unroll 1
    for (int it = 0; it < MAXSPIN; ++it) {
      const u32 m = (yv[0][0] ^ tw) | (yv[0][1] ^ tw) | (yv[0][2] ^ tw) | (yv[0][3] ^ tw) | (yv[1][0] ^ tw) | (yv[1][1] ^ tw) | (yv[1][2] ^ tw) | (yv[1][3] ^ tw);
      if (__all(m < 0x10000u)) break;
      __builtin_amdgcn_s_sleep(4);
      loadY();
    }
    DBGCNT(st_[3] = (u32)__builtin_amdgcn_s_memrealtime();)
    const f32x4* rr = (const f32x4*)(lds + A_RED + (w ^ 1) * 2048) + lane;
    const float gg = bflo(gv), og = rbf(1.f - gg);
    u32 res[2][2];
#pragma unroll
    for (int m = 0; m < 2; ++m) {
      f32x4 a = rr[m * 64];
      a += m ? keep1 : keep0;
      float r[4];
#pragma unroll
      for (int i = 0; i < 4; ++i) {
        float av_ = rbf(a[i]);
        r[i] = rbf(smul(gg, av_)) + rbf(smul(og, bflo(yv[m][i])));
      }
      res[m][0] = pk2(r[0], r[1]); res[m][1] = pk2(r[2], r[3]);
    }
    u32x4 ov;
    {
      auto s0 = __builtin_amdgcn_permlane32_swap(res[0][0], res[1][0], false, false);
      auto s1 = __builtin_amdgcn_permlane32_swap(res[0][1], res[1][1], false, false);
      auto t0 = __builtin_amdgcn_permlane16_swap(s0[0], s0[1], false, false);
      auto t1 = __builtin_amdgcn_permlane16_swap(s1[0], s1[1], false, false);
      ov = (u32x4){t0[0], t1[0], t0[1], t1[1]};
    }
    *(u32x4*)(p.out + (size_t)ftok * D + c0 + 32 * kh + 8 * g) = ov;
    STAMP(11);
  }
  else if (x8 == 0 && j == 31) {
    if (tid < 256) {
      const u32 off = tid < 224 ? WS_YT + 2 * tid * (S * 4) : WS_OF + ((tid - 224) & 15) * 1024 + 12;
#pragma unroll 1
      for (int it = 0; it < MAXSPIN; ++it) {
        const u32 v = ld4c(rWS, off, 0);
        if (__all((v >> 16) == t16)) break;
        __builtin_amdgcn_s_sleep(16);
      }
    }
    CFENCE();
    BAR();
    if (tid == 0) __hip_atomic_store((u32*)(ws + WS_GEN), tag, __ATOMIC_RELAXED, __HIP_MEMORY_SCOPE_AGENT);
  }
  LFLUSH(ws);
  STAMP_FLUSH(ws);
}

DI f32x2 cmul(f32x2 a, f32x2 b) {
  float t0, t1, re, im;
  asm("v_mul_f32 %0, %1, %2" : "=v"(t0) : "v"(a.y), "v"(b.y));
  asm("v_mul_f32 %0, %1, %2" : "=v"(t1) : "v"(a.y), "v"(b.x));
  asm("v_fma_f32 %0, %1, %2, -%3" : "=v"(re) : "v"(a.x), "v"(b.x), "v"(t0));
  asm("v_fma_f32 %0, %1, %2, %3" : "=v"(im) : "v"(a.x), "v"(b.y), "v"(t1));
  return (f32x2){re, im};
}
DI f32x2 cexp_pow(float mr, float pr, float a) {
  float m = __builtin_amdgcn_exp2f(mr * a);
  float t = __builtin_amdgcn_fractf(pr * a);
  return (f32x2){m * __builtin_amdgcn_cosf(t), m * __builtin_amdgcn_sinf(t)};
}
constexpr int KG_ROW = 272, KG_SLAB = 16 * KG_ROW;
constexpr int B_UC = 34816, UCH = 16656, B_KR = 68128, B_KTAB = 77344, B_DUM = 80416, B_TIMG = B_KR, B_RED = 0;
constexpr int UC0 = 8, UC1 = 4176, UC2 = 8336, UC3 = 12496;

DI void roleB(const Params& p, int x8, int jb, char* lds) {
  const int tid = threadIdx.x, lane = tid & 63, w = __builtin_amdgcn_readfirstlane(tid >> 6);
  const int g = lane >> 4, l15 = lane & 15;
  char* ws = p.ws;
  STAMP_DECL
  STAMP(0);
  __builtin_amdgcn_s_setprio(3);
  const rsrc_t rWS = mkrs(ws, WS_END);
  const int tt = 4 * x8 + jb / 7, cg = jb % 7, ch0 = 2 * (x8 * 28 + jb);
  const u32 gen = sload(ws + WS_GEN);
  if (w == 7) {
    const u32x4 rc = mkrs4((const void*)(__builtin_amdgcn_s_getpc() & ~(unsigned long long)127), CODE_BYTES);
#pragma unroll
    for (int i = 0; i < (CODE_BYTES + 8191) / 8192; ++i) atouch4(rc, ldsaddr(lds) + B_DUM, lane * 128, i * 8192);
  } else if (w >= 5) {
    atouch4(mkrs4(p.in[3], D * D * 2), ldsaddr(lds) + B_DUM, lane * 128, (jb * 2 + (w - 5)) * 7168);
  }
  const u32x4 xv = *(const u32x4*)(p.in[0] + (size_t)(tt * 64 + (tid >> 3)) * D + cg * 64 + (tid & 7) * 8);
  const int d = ch0 + (w >> 2);
  const int mo = d * NM + (w & 3) * 64 + lane;
  const u32 are = p.in[5][mo], aim = p.in[6][mo], bre = p.in[7][mo], bim = p.in[8][mo], cre = p.in[9][mo], cim = p.in[10][mo];
  const u32 ldt = p.in[11][d], dskip = p.in[12][d];
  __builtin_amdgcn_sched_barrier(0);
  const u32 tag = gen + 1u, tw = (tag & 0xffffu) << 16;
  {
    char* img = lds + B_TIMG;
    u32* dst = (u32*)(img + (tid >> 3) * 132 + (tid & 7) * 16);
    dst[0] = xv[0]; dst[1] = xv[1]; dst[2] = xv[2]; dst[3] = xv[3];
    bar_l();
    const int chl = tid >> 3, oc = tid & 7;
    u32 o[8];
#pragma unroll
    for (int jj = 0; jj < 8; ++jj) o[jj] = *(const u16*)(img + (4 * oc + (jj & 3) + 32 * (jj >> 2)) * 132 + chl * 2) | tw;
    st16c((u32x4){o[0], o[1], o[2], o[3]}, rWS, (cg * 64 + chl) * (S * 4) + tt * 256 + oc * 16, WS_XT);
    st16c((u32x4){o[4], o[5], o[6], o[7]}, rWS, (cg * 64 + chl) * (S * 4) + tt * 256 + oc * 16 + 128, WS_XT);
  }
  __builtin_amdgcn_s_setprio(BPRIO);
  STAMP(1);
  char* slab = lds + w * KG_SLAB;
  const int cch = tid >> 8, cq = tid & 255;
  u32x4 ua, ub, na, nb;
  const u32 uvo = (ch0 + cch) * (S * 4) + cq * 16;
  auto loadU = [&]() {
    ua = ld16c(rWS, uvo, WS_XT); ub = ld16c(rWS, uvo + 4096, WS_XT);
    na = ua; nb = ub;
    if (lane == 63) { na = ld16c(rWS, uvo + 16, WS_XT); nb = ld16c(rWS, cq < 255 ? uvo + 4096 + 16 : uvo, WS_XT); }
  };
  f32x4 acc[3];
  {
    const float dt = rbf(__expf(bflo(ldt)));
    const float ar = dt * bflo(are), ai = dt * bflo(aim), br = dt * bflo(bre), bi = dt * bflo(bim), cr = bflo(cre), ci = bflo(cim);
    const float mr = ar * 1.4426950408889634f, pr = ai * 0.15915494309189535f;
    f32x2 W = {cr * br - ci * bi, -(cr * bi + ci * br)};
    f32x2 z1c = cexp_pow(mr, -pr, 1.f);
    f32x2 z8 = cexp_pow(mr, pr, 8.f);
    f32x2 a128 = cexp_pow(mr, -pr, 128.f);
    asm volatile("s_mov_b32 m0, %0" ::"s"(ldsaddr(slab)) : "memory");
#define WROWT(r, v) do { const f32x2 v_ = (v); asm volatile("ds_write_addtid_b32 %0 offset:%1" ::"v"(pk2(v_.x, v_.y)), "i"((r) * KG_ROW) : "memory"); } while (0)
    const char* rd = slab + l15 * KG_ROW + g * 16;
    u32x4 yf[4];
    {
      f32x2 y[16];
      y[1] = z8;
      f32x2 y1c = z8; asm volatile("" : "+v"(y1c));
      y[2] = cmul(y[1], y1c); y[3] = cmul(y[2], y[1]); y[4] = cmul(y[3], y[1]);
      y[5] = cmul(y[4], y[1]); y[6] = cmul(y[4], y[2]); y[7] = cmul(y[4], y[3]); y[8] = cmul(y[5], y[3]);
#pragma unroll
      for (int c = 9; c < 16; ++c) y[c] = cmul(y[8], y[c - 8]);
      WROWT(0, ((f32x2){1.f, 0.f}));
#pragma unroll
      for (int c = 1; c < 16; ++c) WROWT(c, y[c]);
    }
#pragma unroll
    for (int ks = 0; ks < 4; ++ks) yf[ks] = *(const u32x4*)(rd + ks * 64);
    CFENCE();
    f32x2 R0[8];
    {
      f32x2 zc = z1c; asm volatile("" : "+v"(zc));
      f32x2 z2 = cmul(z1c, zc);
      f32x2 z2c = z2; asm volatile("" : "+v"(z2c));
      f32x2 z4 = cmul(z2, z2c);
      R0[0] = W; R0[1] = cmul(W, z1c); R0[2] = cmul(W, z2); R0[3] = cmul(R0[1], z2);
#pragma unroll
      for (int b = 0; b < 4; ++b) R0[4 + b] = cmul(R0[b], z4);
    }
    f32x2 A[17];
    {
      A[1] = a128;
      f32x2 a1c = a128; asm volatile("" : "+v"(a1c));
      A[2] = cmul(A[1], a1c); A[3] = cmul(A[2], A[1]); A[4] = cmul(A[3], A[1]);
      A[5] = cmul(A[4], A[1]); A[6] = cmul(A[4], A[2]); A[7] = cmul(A[4], A[3]); A[8] = cmul(A[5], A[3]);
#pragma unroll
      for (int m = 9; m < 16; ++m) A[m] = cmul(A[8], A[m - 8]);
      A[16] = cmul(A[9], A[7]);
    }
    {
      f32x2 t[8];
#pragma unroll
      for (int b = 0; b < 8; ++b) t[b] = cmul(R0[b], A[1]);
#pragma unroll
      for (int b = 0; b < 8; ++b) { WROWT(b, R0[b]); WROWT(8 + b, t[b]); }
    }
    acc[0] = (f32x4){0, 0, 0, 0};
#pragma unroll
    for (int ks = 0; ks < 4; ++ks) { u32x4 xf = *(const u32x4*)(rd + ks * 64); acc[0] = mfma16(xf, yf[ks], acc[0]); }
    CFENCE();
    STAMP(7);
    {
      f32x2 t[16];
#pragma unroll
      for (int hh = 0; hh < 2; ++hh)
#pragma unroll
        for (int b = 0; b < 4; ++b) t[b + 4 * hh] = cmul(R0[2 * b], A[2 + hh]);
#pragma unroll
      for (int hh = 0; hh < 4; ++hh)
#pragma unroll
        for (int b = 0; b < 2; ++b) t[8 + b + 2 * hh] = cmul(R0[4 * b], A[4 + hh]);
#pragma unroll
      for (int r = 0; r < 16; ++r) WROWT(r, t[r]);
    }
    acc[1] = (f32x4){0, 0, 0, 0};
#pragma unroll
    for (int ks = 0; ks < 4; ++ks) { u32x4 xf = *(const u32x4*)(rd + ks * 64); acc[1] = mfma16(xf, yf[ks], acc[1]); }
    CFENCE();
    STAMP(9);
    __builtin_amdgcn_s_setprio(3);
    loadU();
    __builtin_amdgcn_s_setprio(BPRIO);
    __builtin_amdgcn_sched_barrier(0);
    {
      f32x2 t[9];
#pragma unroll
      for (int hh = 0; hh < 9; ++hh) t[hh] = cmul(W, A[8 + hh]);
#pragma unroll
      for (int hh = 0; hh < 9; ++hh) WROWT(hh, t[hh]);
    }
    acc[2] = (f32x4){0, 0, 0, 0};
#pragma unroll
    for (int ks = 0; ks < 4; ++ks) { u32x4 xf = *(const u32x4*)(rd + ks * 64); acc[2] = mfma16(xf, yf[ks], acc[2]); }
    CFENCE();
    float* kt = (float*)slab;
    *(f32x4*)(kt + 8 * l15 + 4 * (g & 1) + 128 * (g >> 1)) = acc[0];
    {
      const int t0 = g < 2 ? 256 + 4 * l15 + 64 * g : 384 + 2 * l15 + 64 * (g - 2);
      const int t1 = g < 2 ? t0 + 2 : t0 + 32;
      *(f32x2*)(kt + t0) = (f32x2){acc[1][0], acc[1][1]};
      *(f32x2*)(kt + t1) = (f32x2){acc[1][2], acc[1][3]};
    }
#pragma unroll
    for (int i = 0; i < 4; ++i) kt[512 + l15 + 16 * (4 * g + i)] = acc[2][i];
  }
  STAMP(2);
  {
    __builtin_amdgcn_s_setprio(3);
#pragma unroll 1
    for (int it = 0; it < MAXSPIN; ++it) {
      const u32 m = (ua[0] ^ tw) | (ua[1] ^ tw) | (ua[2] ^ tw) | (ua[3] ^ tw) | (ub[0] ^ tw) | (ub[1] ^ tw) | (ub[2] ^ tw) | (ub[3] ^ tw) | (na[0] ^ tw) | (na[1] ^ tw) | (na[2] ^ tw) | (nb[0] ^ tw) | (nb[1] ^ tw) | (nb[2] ^ tw);
      if (__all(m < 0x10000u)) break;
      __builtin_amdgcn_s_sleep(2);
      loadU();
    }
    __builtin_amdgcn_s_setprio(BPRIO);
    STAMP(10);
    char* base = lds + B_UC + cch * UCH + (16 + cq * 4) * 2;
    u32 e00, sh00;
#pragma unroll
    for (int hf = 0; hf < 2; ++hf) {
      const u32x4 uu = hf ? ub : ua, nn = hf ? nb : na;
      const u32 e0 = __builtin_amdgcn_perm(uu[1], uu[0], 0x05040100u), e1 = __builtin_amdgcn_perm(uu[3], uu[2], 0x05040100u);
      u32 f0 = __builtin_amdgcn_perm(nn[1], nn[0], 0x05040100u), f1 = nn[2] & 0xffffu;
      if (hf == 1 && cq == 255) { f0 = 0; f1 = 0; }
      const u32 n0 = __builtin_amdgcn_update_dpp(f0, e0, 0x130, 0xf, 0xf, false), n1 = __builtin_amdgcn_update_dpp(f1, e1, 0x130, 0xf, 0xf, false);
      const u32 s0 = __builtin_amdgcn_alignbit(e1, e0, 16), s1 = __builtin_amdgcn_alignbit(n0, e1, 16), s2 = __builtin_amdgcn_alignbit(n1, n0, 16);
      char* bh = base + hf * 2048;
      *(u32x2*)(bh + UC0) = (u32x2){e0, e1};
      *(u32x2*)(bh + UC1) = (u32x2){s0, s1};
      *(u32x2*)(bh + UC2) = (u32x2){e1, n0};
      *(u32x2*)(bh + UC3) = (u32x2){s1, s2};
      if (hf == 0) { e00 = e0; sh00 = s0; }
    }
    if (cq == 0) {
      char* b0 = lds + B_UC + cch * UCH;
      const u32x4 z = {0, 0, 0, 0};
      *(u32x2*)(b0 + UC0) = (u32x2){0, 0}; *(u32x2*)(b0 + UC0 + 8) = (u32x2){0, 0}; *(u32x2*)(b0 + UC0 + 16) = (u32x2){0, 0}; *(u32x2*)(b0 + UC0 + 24) = (u32x2){0, 0};
      *(u32x4*)(b0 + UC1) = z; *(u32x4*)(b0 + UC1 + 16) = (u32x4){0, 0, 0, e00 << 16};
      *(u32x4*)(b0 + UC2) = z; *(u32x4*)(b0 + UC2 + 16) = (u32x4){0, 0, 0, e00};
      *(u32x4*)(b0 + UC3) = z; *(u32x4*)(b0 + UC3 + 16) = (u32x4){0, 0, e00 << 16, sh00};
    }
  }
  __builtin_amdgcn_s_setprio(BPRIO2);
  bar_l();
  STAMP(3);
  if (tid < 384) {
    const int ch = tid >= 192 ? 1 : 0, t4 = (tid - 192 * ch) * 4;
    const float* k0 = (const float*)(lds + 4 * ch * KG_SLAB) + t4;
    f32x4 s = *(const f32x4*)k0;
#pragma unroll
    for (int q = 1; q < 4; ++q) s += *(const f32x4*)(k0 + q * (KG_SLAB / 4));
    *(u32x2*)(lds + B_KTAB + ch * 1536 + t4 * 2) = (u32x2){pk2(s[0], s[1]), pk2(s[2], s[3])};
  }
  bar_l();
  {
    const int jhi = 16 * (cq >> 1) + 15 - (cq & 1) * 8;
    const float fj = (float)jhi;
    const char* kt = lds + B_KTAB + cch * 1536;
    u32 o[4];
#pragma unroll
    for (int e = 0; e < 4; ++e) {
      u32 bits = pk2(fj - (float)(2 * e), fj - (float)(2 * e + 1));
      u32 t0 = (bits & 0xffffu) - 0x4280u, t1 = (bits >> 16) - 0x4280u;
      if (jhi < 256) { t0 = jhi - 2 * e; t1 = jhi - 2 * e - 1; }
      u32 lo = *(const u16*)(kt + t0 * 2), hi = *(const u16*)(kt + t1 * 2);
      o[e] = lo | (hi << 16);
    }
    char* kr = lds + B_KR + cch * 4608;
    *(u32x4*)(kr + 512 + cq * 16) = (u32x4){o[0], o[1], o[2], o[3]};
    if (cq < 32) *(u32x4*)(kr + cq * 16) = (u32x4){0, 0, 0, 0};
  }
  bar_l();
  STAMP(4);
  {
    const int ch = w >> 2, rh = (w >> 1) & 1, sh = w & 1;
    const int idx0 = 8 * g + l15 + 1, cc = idx0 & 3;
    const u32 ucb = cc == 0 ? UC0 : (cc == 1 ? UC1 : (cc == 2 ? UC2 : UC3));
    const LAS char* abase = (const LAS char*)(lds + B_UC + ch * UCH + ucb + (idx0 & ~3) * 2 + rh * 256);
    const char* bbase = lds + B_KR + ch * 4608 + (256 + 16 * (l15 - (g >> 1)) + 8 * (g & 1)) * 2 - rh * 256;
    f32x4 c4[4];
#pragma unroll
    for (int i = 0; i < 4; ++i) c4[i] = (f32x4){0, 0, 0, 0};
    if (sh == 0) {
#pragma unroll 1
      for (int rr = 0; rr < 4; ++rr) {
        u32x4 af[8], bf[8];
#pragma unroll
        for (int a = 0; a < 8; ++a) {
          const LAS char* pa = abase + (256 * a) * 2 + rr * 64;
          u32x2 lo = *(const volatile LAS u32x2*)pa, hi = *(const volatile LAS u32x2*)(pa + 8);
          af[a] = (u32x4){lo[0], lo[1], hi[0], hi[1]};
          bf[a] = *(const u32x4*)(bbase + (256 * a) * 2 - rr * 64);
        }
        c4[0] = mfma16(af[0], bf[0], c4[0]);
        c4[1] = mfma16(af[0], bf[1], c4[1]); c4[1] = mfma16(af[1], bf[0], c4[1]);
#pragma unroll
        for (int a = 0; a < 8; ++a) {
          if (a < 7) c4[2] = mfma16(af[a], bf[6 - a], c4[2]);
          c4[3] = mfma16(af[a], bf[7 - a], c4[3]);
        }
      }
    } else {
#pragma unroll 1
      for (int rr = 0; rr < 4; ++rr) {
        u32x4 af[6], bf[6];
#pragma unroll
        for (int a = 0; a < 6; ++a) {
          const LAS char* pa = abase + (256 * a) * 2 + rr * 64;
          u32x2 lo = *(const volatile LAS u32x2*)pa, hi = *(const volatile LAS u32x2*)(pa + 8);
          af[a] = (u32x4){lo[0], lo[1], hi[0], hi[1]};
          bf[a] = *(const u32x4*)(bbase + (256 * a) * 2 - rr * 64);
        }
#pragma unroll
        for (int a = 0; a < 6; ++a) {
          if (a < 3) c4[0] = mfma16(af[a], bf[2 - a], c4[0]);
          if (a < 4) c4[1] = mfma16(af[a], bf[3 - a], c4[1]);
          if (a < 5) c4[2] = mfma16(af[a], bf[4 - a], c4[2]);
          c4[3] = mfma16(af[a], bf[5 - a], c4[3]);
        }
      }
    }
    STAMP(5);
    const f32x4 e0 = c4[0], e1 = c4[1], e2 = c4[2], e3 = c4[3];
    f32x4* xw = (f32x4*)(lds + B_RED + w * 2048) + lane;
    xw[0] = rh ? e0 : e2; xw[64] = rh ? e1 : e3;
    bar_l();
    const f32x4* xr = (const f32x4*)(lds + B_RED + (w ^ 2) * 2048) + lane;
    f32x4 v0 = xr[0], v1 = xr[64];
    v0 += rh ? e2 : e0; v1 += rh ? e3 : e1;
    const int pt = sh == 0 ? (rh ? 6 : 0) : (rh ? 4 : 2);
    char* strip = lds + B_RED + 16384 + w * 1024;
    *(u32x2*)(strip + (4 * l15 + g) * 8) = (u32x2){pk2(v0[0], v0[1]), pk2(v0[2], v0[3])};
    *(u32x2*)(strip + 512 + (4 * l15 + g) * 8) = (u32x2){pk2(v1[0], v1[1]), pk2(v1[2], v1[3])};
    const char* ub0 = lds + B_UC + ch * UCH + UC0 + (16 + 256 * pt + 4 * lane) * 2;
    const u32x2 x0 = *(const u32x2*)ub0, x1 = *(const u32x2*)(ub0 + 512);
    const float dsk = bflo(dskip);
    CFENCE();
    WAIT_L(0);
    const u32x2 y0 = *(const u32x2*)(strip + lane * 8), y1 = *(const u32x2*)(strip + 512 + lane * 8);
    __builtin_amdgcn_s_setprio(3);
    const u32 yo = (ch0 + ch) * (S * 4) + (256 * pt + 4 * lane) * 4;
#pragma unroll
    for (int hf = 0; hf < 2; ++hf) {
      const u32x2 xx = hf ? x1 : x0, yy = hf ? y1 : y0;
      const u32 pa = pk2(smul(bflo(xx[0]), dsk), smul(bfhi(xx[0]), dsk)), pb = pk2(smul(bflo(xx[1]), dsk), smul(bfhi(xx[1]), dsk));
      const u32 ra = pk2(bflo(yy[0]) + bflo(pa), bfhi(yy[0]) + bfhi(pa)), rb = pk2(bflo(yy[1]) + bflo(pb), bfhi(yy[1]) + bfhi(pb));
      st16c((u32x4){(ra & 0xffffu) | tw, (ra >> 16) | tw, (rb & 0xffffu) | tw, (rb >> 16) | tw}, rWS, yo + hf * 1024, WS_YT);
    }
    STAMP(6);
  }
  STAMP_FLUSH(ws);
}

__global__ __launch_bounds__(512, 4) void fused(Params p) {
  __shared__ __attribute__((aligned(16))) char lds[LDS_BYTES];
  const int b = blockIdx.x, x8 = b & 7, j = b >> 3;
  if (j < 32) roleA(p, x8, j, lds);
  else roleB(p, x8, j - 32, lds);
}

extern "C" void kernel_launch(void* const* d_in, const int* in_sizes, int n_in, void* d_out, int out_size, void* d_ws, size_t ws_size, hipStream_t stream) {
  Params p;
  for (int i = 0; i < 14; ++i) p.in[i] = (const u16*)d_in[i];
  p.ws = (char*)d_ws; p.out = (u16*)d_out;
  fused<<<480, 512, 0, stream>>>(p);
}
```

```cpp
#include <hip/hip_runtime.h>
#include <stdint.h>

typedef unsigned short u16;
typedef unsigned int u32;
typedef short s16x4 __attribute__((ext_vector_type(4)));
typedef __bf16 bf16x8 __attribute__((ext_vector_type(8)));
typedef __bf16 bf16x2 __attribute__((ext_vector_type(2)));
typedef float f32x4 __attribute__((ext_vector_type(4)));
typedef float f32x2 __attribute__((ext_vector_type(2)));
typedef u32 u32x4 __attribute__((ext_vector_type(4)));
typedef u32 u32x2 __attribute__((ext_vector_type(2)));
typedef __amdgpu_buffer_rsrc_t rsrc_t;
#define LAS __attribute__((address_space(3)))
#define GAS __attribute__((address_space(1)))
#define DI __device__ __forceinline__

constexpr int S = 2048, D = 448, H = 16, DH = 28, NM = 256;

constexpr u32 WS_GEN = 0;
constexpr u32 WS_FLAG = 4096, FSTR = 128;
constexpr u32 F_B = 0;
constexpr u32 F_A = 224;
constexpr u32 NFLAG = F_A + 256;
constexpr u32 WS_XT = 1u << 20;
constexpr u32 WS_KVP = WS_XT + D * S * 4;
constexpr u32 WS_OF = WS_KVP + 16 * 16 * 4 * 1024;
constexpr u32 WS_YT = WS_OF + 128 * 16 * 1024;
constexpr u32 WS_GATE = WS_YT + D * S * 4;
constexpr u32 WS_END = WS_GATE + S * 4;
static_assert(WS_FLAG + NFLAG * FSTR <= WS_XT, "flags");
constexpr int MAXSPIN = 400000;
#define CODE_BYTES 8192

#define STAMP(i)
#define STAMP_DECL
#define STAMP_FLUSH(ws)

DI float bflo(u32 v) { return __uint_as_float(v << 16); }
DI float bfhi(u32 v) { return __uint_as_float(v & 0xffff0000u); }
DI u32 pk2(float lo, float hi) { f32x2 p = {lo, hi}; bf16x2 q = __builtin_convertvector(p, bf16x2); return __builtin_bit_cast(u32, q); }
DI float smul(float a, float b) { float r; asm("v_mul_f32 %0, %1, %2" : "=v"(r) : "v"(a), "v"(b)); return r; }
DI float rbf(float f) { return bflo(pk2(f, f)); }
DI f32x4 mfma16(u32x4 a, u32x4 b, f32x4 c) {
  return __builtin_amdgcn_mfma_f32_16x16x32_bf16(__builtin_bit_cast(bf16x8, a), __builtin_bit_cast(bf16x8, b), c, 0, 0, 0);
}
DI u32x2 trread(const char* lds_base, u32 off) {
  s16x4 t = __builtin_amdgcn_ds_read_tr16_b64_v4i16((LAS s16x4*)((LAS char*)lds_base + off));
  return __builtin_bit_cast(u32x2, t);
}
DI rsrc_t mkrs(const void* p, u32 bytes) { return __builtin_amdgcn_make_buffer_rsrc((void*)p, 0, bytes, 0x00020000); }
DI void dma16(rsrc_t r, char* lds_dst, u32 voff, u32 soff) { __builtin_amdgcn_raw_ptr_buffer_load_lds(r, (LAS void*)lds_dst, 16, voff, soff, 0, 0); }
DI void touch4(rsrc_t r, char* lds_dst, u32 voff, u32 soff) { __builtin_amdgcn_raw_ptr_buffer_load_lds(r, (LAS void*)lds_dst, 4, voff, soff, 0, 0); }
DI u32x4 mkrs4(const void* p, u32 bytes) { unsigned long long a = (unsigned long long)p; return (u32x4){(u32)a, (u32)(a >> 32) & 0xffffu, bytes, 0x00020000u}; }
DI u32 ldsaddr(const char* p) { return (u32)(uintptr_t)(LAS const char*)p; }
DI void adma16(u32x4 rs, u32 lds_dst, u32 voff, u32 soff) {
  asm volatile("s_mov_b32 m0, %0\n\ts_nop 0\n\tbuffer_load_dwordx4 %1, %2, %3 offen lds" ::"s"(lds_dst), "v"(voff), "s"(rs), "s"(soff) : "memory");
}
DI void adma16h(u32x4 rs, u32 lds_dst, u32 voff, u32 soff) {
  asm volatile("s_mov_b32 m0, %0\n\ts_mov_b32 exec_hi, 0\n\tbuffer_load_dwordx4 %1, %2, %3 offen lds\n\ts_mov_b32 exec_hi, -1" ::"s"(lds_dst), "v"(voff), "s"(rs), "s"(soff) : "memory");
}
DI u32 sload(const void* p) { return *(const __attribute__((address_space(4))) u32*)(unsigned long long)p; }
DI void atouch4(u32x4 rs, u32 lds_dst, u32 voff, u32 soff) {
  asm volatile("s_mov_b32 m0, %0\n\ts_nop 0\n\tbuffer_load_dword %1, %2, %3 offen lds" ::"s"(lds_dst), "v"(voff), "s"(rs), "s"(soff) : "memory");
}
DI u32x4 ald16(u32x4 rs, u32 voff, u32 soff) {
  u32x4 r; asm volatile("buffer_load_dwordx4 %0, %1, %2, %3 offen" : "=v"(r) : "v"(voff), "s"(rs), "s"(soff) : "memory"); return r;
}
template <int N> DI void waitv4(u32x4& a, u32x4& b, u32x4& c, u32x4& d) { asm volatile("s_waitcnt vmcnt(%4)" : "+v"(a), "+v"(b), "+v"(c), "+v"(d) : "i"(N) : "memory"); }
DI u32x4 ld16(rsrc_t r, u32 voff, u32 soff) { return __builtin_amdgcn_raw_buffer_load_b128(r, voff, soff, 0); }
DI u32x4 ld16c(rsrc_t r, u32 voff, u32 soff) { return __builtin_amdgcn_raw_buffer_load_b128(r, voff, soff, 16); }
DI u32x2 ld8c(rsrc_t r, u32 voff, u32 soff) { return __builtin_amdgcn_raw_buffer_load_b64(r, voff, soff, 16); }
DI u32 ld4c(rsrc_t r, u32 voff, u32 soff) { return __builtin_amdgcn_raw_buffer_load_b32(r, voff, soff, 16); }
DI u32 ld2c(rsrc_t r, u32 voff, u32 soff) { return __builtin_amdgcn_raw_buffer_load_b16(r, voff, soff, 16); }
DI void st16c(u32x4 v, rsrc_t r, u32 voff, u32 soff) { __builtin_amdgcn_raw_buffer_store_b128(v, r, voff, soff, 16); }
DI void st4c(u32 v, rsrc_t r, u32 voff, u32 soff) { __builtin_amdgcn_raw_buffer_store_b32(v, r, voff, soff, 16); }
#define WAIT_L(n) asm volatile("s_waitcnt lgkmcnt(" #n ")" ::: "memory")
#define BAR() __builtin_amdgcn_s_barrier()
#define CFENCE() asm volatile("" ::: "memory")
template <int N> DI void waitv() { asm volatile("s_waitcnt vmcnt(%0)" ::"i"(N) : "memory"); }
DI void bar_l() { WAIT_L(0); BAR(); CFENCE(); }

DI u32 flag_off(u32 idx) { return WS_FLAG + idx * FSTR; }
DI void publish(char* ws, u32 idx, u32 tag, int lane) {
  waitv<0>();
  if (lane == 0) __hip_atomic_store((u32*)(ws + flag_off(idx)), tag, __ATOMIC_RELAXED, __HIP_MEMORY_SCOPE_AGENT);
}
DI void pollf(char* ws, u32 idx, u32 tag) {
  const u32* f = (const u32*)(ws + flag_off(idx));
#pragma unroll 1
  for (int it = 0; it < MAXSPIN; ++it) {
    u32 v = __hip_atomic_load(f, __ATOMIC_RELAXED, __HIP_MEMORY_SCOPE_AGENT);
    if (__all(v == tag)) break;
    __builtin_amdgcn_s_sleep(1);
  }
  CFENCE();
}

DI float red_max_g(float x) {
  u32 u = __float_as_uint(x);
  auto r = __builtin_amdgcn_permlane16_swap(u, u, false, false);
  x = fmaxf(__uint_as_float(r[0]), __uint_as_float(r[1]));
  u = __float_as_uint(x);
  auto r2 = __builtin_amdgcn_permlane32_swap(u, u, false, false);
  return fmaxf(__uint_as_float(r2[0]), __uint_as_float(r2[1]));
}
DI float red_sum_g(float x) {
  u32 u = __float_as_uint(x);
  auto r = __builtin_amdgcn_permlane16_swap(u, u, false, false);
  x = __uint_as_float(r[0]) + __uint_as_float(r[1]);
  u = __float_as_uint(x);
  auto r2 = __builtin_amdgcn_permlane32_swap(u, u, false, false);
  return __uint_as_float(r2[0]) + __uint_as_float(r2[1]);
}
template <int CTRL> DI float dpp_add(float x) {
  return x + __uint_as_float(__builtin_amdgcn_update_dpp(0, __float_as_uint(x), CTRL, 0xf, 0xf, false));
}
DI float red_sum_all(float x) {
  x = dpp_add<0x128>(x); x = dpp_add<0x124>(x); x = dpp_add<0x122>(x); x = dpp_add<0x121>(x);
  return red_sum_g(x);
}

#define NPRE 3
#define LDECL
#define LST(k)
#define LST2(k)
#define LFLUSH(ws)
#define P2SLEEP 2
#define DBGCNT(x)
#define LD1 ld16
#define LD2 ld16
#define BPRIO2 0
#define APRIO2 2
#define APRIO 0
#define BPRIO 2
DI float amax3(float a, float b, float c) { float r; asm("v_max3_f32 %0, %1, %2, %3" : "=v"(r) : "v"(a), "v"(b), "v"(c)); return r; }
DI float anmin3(float a, float b, float c) { float r; asm("v_max3_f32 %0, -%1, -%2, -%3" : "=v"(r) : "v"(a), "v"(b), "v"(c)); return r; }
DI float amax2(float a, float b) { float r; asm("v_max_f32 %0, %1, %2" : "=v"(r) : "v"(a), "v"(b)); return r; }
template <bool MAX> DI void red2_g(float& a, float& b) {
  auto r = __builtin_amdgcn_permlane16_swap(__float_as_uint(a), __float_as_uint(b), false, false);
  float t = MAX ? amax2(__uint_as_float(r[0]), __uint_as_float(r[1])) : __uint_as_float(r[0]) + __uint_as_float(r[1]);
  auto r2 = __builtin_amdgcn_permlane32_swap(__float_as_uint(t), __float_as_uint(t), false, false);
  t = MAX ? amax2(__uint_as_float(r2[0]), __uint_as_float(r2[1])) : __uint_as_float(r2[0]) + __uint_as_float(r2[1]);
  auto r3 = __builtin_amdgcn_permlane16_swap(__float_as_uint(t), __float_as_uint(t), false, false);
  a = __uint_as_float(r3[0]); b = __uint_as_float(r3[1]);
}
DI void dual_softmax(const f32x4 (&pacc)[2], u32x4& pos, u32x4& neg) {
  constexpr float L2E = 1.4426950408889634f;
  float p[7];
  {
    u32 a = pk2(pacc[0][0], pacc[0][1]), b = pk2(pacc[0][2], pacc[0][3]), c = pk2(pacc[1][0], pacc[1][1]), d = pk2(pacc[1][2], pacc[1][2]);
    p[0] = bflo(a); p[1] = bfhi(a); p[2] = bflo(b); p[3] = bfhi(b); p[4] = bflo(c); p[5] = bfhi(c); p[6] = bflo(d);
  }
  float mx = amax3(amax3(p[0], p[1], p[2]), amax3(p[3], p[4], p[5]), p[6]);
  float nm = amax2(amax2(anmin3(p[0], p[1], p[2]), anmin3(p[3], p[4], p[5])), -p[6]);
  red2_g<true>(mx, nm);
  const f32x2 cc = {L2E, -L2E}, off = {-mx * L2E, -nm * L2E};
  f32x2 e[7];
#pragma unroll
  for (int i = 0; i < 7; ++i) {
    const f32x2 a = __builtin_elementwise_fma((f32x2){p[i], p[i]}, cc, off);
    e[i] = (f32x2){__builtin_amdgcn_exp2f(a.x), __builtin_amdgcn_exp2f(a.y)};
  }
  const f32x2 sm = ((e[0] + e[1]) + (e[2] + e[3])) + ((e[4] + e[5]) + e[6]);
  float sp = sm.x, sn = sm.y;
  red2_g<false>(sp, sn);
  const f32x2 rr = {__builtin_amdgcn_rcpf(sp), __builtin_amdgcn_rcpf(sn)};
#pragma unroll
  for (int i = 0; i < 7; ++i) e[i] *= rr;
#pragma unroll
  for (int i = 0; i < 3; ++i) { pos[i] = pk2(e[2 * i].x, e[2 * i + 1].x); neg[i] = pk2(e[2 * i].y, e[2 * i + 1].y); }
  pos[3] = pk2(e[6].x, 0.f); neg[3] = pk2(e[6].y, 0.f);
}
DI float acc_sel(u32 v, u32 sel, float a) { return __builtin_amdgcn_fdot2_f32_bf16(__builtin_bit_cast(bf16x2, v), __builtin_bit_cast(bf16x2, sel), a, false); }
DI bool tag_ok(u32 v3, u32 t16) { return __all((v3 >> 16) == t16); }

struct Params { const u16* in[14]; char* ws; u16* out; };

constexpr int A_X = 0, A_W = 32768, A_WPHI = 73728, A_GW = 75776, A_VT = 16384, A_Q6 = 24576, A_KVB = 76800, A_RED = 65536;
constexpr int LDS_BYTES = 81920;
constexpr int WROW = 3 * D * 2;
constexpr int WCH = 64 * WROW;

DI void roleA(const Params& p, int x8, int j, char* lds) {
  const int tid = threadIdx.x, lane = tid & 63, w = __builtin_amdgcn_readfirstlane(tid >> 6);
  const int g = lane >> 4, l15 = lane & 15, q4 = l15 >> 2, pp = lane & 3;
  const int h = 4 * (x8 & 3) + (j & 3), tile = 8 * (x8 >> 2) + (j >> 2);
  const int rot = (2 * (h & 3) + (tile & 7)) % 7;
  char* ws = p.ws;
  LDECL
  STAMP_DECL
  STAMP(0);
  __builtin_amdgcn_s_setprio(3);
  const u32x4 rX = mkrs4(p.in[0], S * D * 2), rW = mkrs4(p.in[1], D * 3 * D * 2);
  const rsrc_t rWS = mkrs(ws, WS_END);
  const u32 lb = ldsaddr(lds);
  const u32 gen = sload(ws + WS_GEN);
  const int tok0 = tile * 128 + w * 16;
  u32 xvo[2];
#pragma unroll
  for (int i = 0; i < 2; ++i) { int row = 8 * i + (lane >> 3); xvo[i] = (tok0 + row) * (D * 2) + (((lane & 7) ^ ((row >> 1) & 7)) * 16); }
  u32 wvo;
  { int row = 16 * (w & 3) + (lane >> 2); int cs = (lane & 3) ^ (2 * ((row >> 3) & 1)); wvo = row * WROW + h * (DH * 2) + (cs < 3 ? cs * 16 : 40); }
  auto cidx = [&](int i) { int c = rot + i; return c >= 7 ? c - 7 : c; };
  auto issueX = [&](int i) {
    const u32 so = cidx(i) * 128, dst = lb + A_X + (i & 1) * 16384 + w * 2048;
    adma16(rX, dst, xvo[0], so); adma16(rX, dst + 1024, xvo[1], so);
  };
  auto issueW = [&](int v) {
    const u32 dst = lb + A_W + (v % 5) * 8192 + w * 1024;
    if (v < 7) adma16(rW, dst, wvo, cidx(v) * WCH + (1 + (w >> 2)) * (D * 2));
    else adma16(rW, dst, wvo, cidx(2 * (v - 7) + (w >> 2)) * WCH);
  };
  if (w < 2) adma16(mkrs4(p.in[2], H * DH * DH * 2), lb + A_WPHI + w * 1024, lane * 16, h * (DH * DH * 2) + w * 1024);
  else if (w == 2) { int c = rot + (lane >> 3); c = c >= 7 ? c - 7 : c; adma16(mkrs4(p.in[13], D * 2), lb + A_GW, lane < 56 ? c * 128 + (lane & 7) * 16 : 4096, 0); }
  issueW(0); issueX(0);
  __builtin_amdgcn_s_setprio(2);
  issueW(1); issueX(1);
  __builtin_amdgcn_s_setprio(1);
  issueW(2);
  __builtin_amdgcn_s_setprio(APRIO);
  __builtin_amdgcn_sched_barrier(0);
  STAMP(1);
  f32x4 ak[2] = {{0, 0, 0, 0}, {0, 0, 0, 0}}, av[2] = {{0, 0, 0, 0}, {0, 0, 0, 0}};
  u32 xoff[2];
#pragma unroll
  for (int ks = 0; ks < 2; ++ks) xoff[ks] = w * 2048 + l15 * 128 + (((4 * ks + g) ^ ((l15 >> 1) & 7)) * 16);
  u32 woff[2];
#pragma unroll
  for (int ct = 0; ct < 2; ++ct) woff[ct] = (8 * g + q4) * 64 + ((ct ^ (g & 1)) * 32) + pp * 8;
  u32x4 xf[14];
  const bool isg = w == (h >> 1) && ((h ^ tile) & 1) == 0;
  f32x4 ga = {0, 0, 0, 0};
  u32x4 wf0[2][2], wf1[2][2];
  auto readX = [&](int i) {
    const char* xs = lds + A_X + (i & 1) * 16384;
    xf[2 * i] = *(const u32x4*)(xs + xoff[0]); xf[2 * i + 1] = *(const u32x4*)(xs + xoff[1]);
  };
  auto readW = [&](int i, int ks, u32x4 (&wf)[2][2]) {
    const char* wsb = lds + A_W + (i % 5) * 8192;
#pragma unroll
    for (int r = 0; r < 2; ++r)
#pragma unroll
      for (int ct = 0; ct < 2; ++ct) {
        u32x2 a = trread(wsb, woff[ct] + r * 4096 + ks * 2048);
        u32x2 b = trread(wsb, woff[ct] + r * 4096 + ks * 2048 + 256);
        wf[r][ct] = (u32x4){a[0], a[1], b[0], b[1]};
      }
  };
  auto mm = [&](const u32x4& x, const u32x4 (&wf)[2][2]) {
#pragma unroll
    for (int ct = 0; ct < 2; ++ct) { ak[ct] = mfma16(wf[0][ct], x, ak[ct]); av[ct] = mfma16(x, wf[1][ct], av[ct]); }
  };
  waitv<3>();
  BAR();
  CFENCE();
  readX(0); readW(0, 0, wf0);
  __builtin_amdgcn_sched_barrier(0);
  readW(0, 1, wf1);
  __builtin_amdgcn_sched_barrier(0);
  WAIT_L(8);
  issueX(2); issueW(3);
#pragma unroll
  for (int i = 0; i < 7; ++i) {
    __builtin_amdgcn_sched_barrier(0);
    mm(xf[2 * i], wf0);
    __builtin_amdgcn_sched_barrier(0);
    if (i < 6) {
      if (i == 0 || i == 3 || i == 4) waitv<4>(); else if (i == 5) { if (w < 4) waitv<3>(); else waitv<2>(); } else waitv<5>();
      LST(2 * i);
      readX(i + 1); readW(i + 1, 0, wf0);
    }
    __builtin_amdgcn_sched_barrier(0);
    mm(xf[2 * i + 1], wf1);
    if (isg) {
      ga = mfma16(*(const u32x4*)(lds + A_GW + i * 128 + g * 16), xf[2 * i], ga);
      ga = mfma16(*(const u32x4*)(lds + A_GW + i * 128 + 64 + g * 16), xf[2 * i + 1], ga);
    }
    __builtin_amdgcn_sched_barrier(0);
    if (i == 1 && w == 7) {
      u32x4 wph[2];
#pragma unroll
      for (int ft = 0; ft < 2; ++ft) {
        int f = 7 * q4 + 4 * ft + pp; f = f > 27 ? 27 : f;
        const char* wp = lds + A_WPHI + f * 2;
        const int d0 = 4 * g, d1 = g < 2 ? 16 + 4 * g : 24;
        u32 e[8];
#pragma unroll
        for (int jj = 0; jj < 4; ++jj) { e[jj] = *(const u16*)(wp + (d0 + jj) * 56); e[4 + jj] = *(const u16*)(wp + (d1 + jj) * 56); }
        wph[ft] = (u32x4){e[0] | (e[1] << 16), e[2] | (e[3] << 16), g == 2 ? 0u : (e[4] | (e[5] << 16)), g == 2 ? 0u : (e[6] | (e[7] << 16))};
      }
      CFENCE();
      *(u32x4*)(lds + A_WPHI + lane * 16) = wph[0]; *(u32x4*)(lds + A_WPHI + 1024 + lane * 16) = wph[1];
      WAIT_L(0);
    }
    if (i < 6) {
      readW(i + 1, 1, wf1);
      __builtin_amdgcn_sched_barrier(0);
      WAIT_L(8);
      if (i + 3 < 7) issueX(i + 3);
      if (i == 0) issueW(4);
      if (i < 5) {
        if (i < 3) waitv<6>();
        LST(2 * i + 1);
        BAR();
        CFENCE();
        LST2(i);
        issueW(i + 5);
        if (i == 4 && w < 4) adma16(rW, lb + A_Q6 + w * 1024, wvo, cidx(6) * WCH);
      }
    }
  }
  __builtin_amdgcn_s_setprio(APRIO2);
  STAMP(2);
  const u32 tag = gen + 1u, t16 = tag & 0xffffu;
  char* phk = lds + A_X; char* vt = lds + A_VT;
  {
    const int tl = w * 16 + l15;
    u32x4 kb = {pk2(ak[0][0], ak[0][1]), pk2(ak[0][2], ak[0][3]), pk2(ak[1][0], ak[1][1]), pk2(ak[1][2], ak[1][3])};
    f32x4 z4 = {0, 0, 0, 0}; f32x4 pk[2];
    pk[0] = mfma16(*(const u32x4*)(lds + A_WPHI + lane * 16), kb, z4); pk[1] = mfma16(*(const u32x4*)(lds + A_WPHI + 1024 + lane * 16), kb, z4);
    u32x4 pos, neg; dual_softmax(pk, pos, neg);
    const int sw = ((tl >> 1) & 1) | (((tl >> 3) & 1) << 1);
    char* rowp = phk + tl * 128 + g * 8;
    *(u32x2*)(rowp + ((0 ^ sw) * 32)) = (u32x2){pos[0], pos[1]};
    *(u32x2*)(rowp + ((1 ^ sw) * 32)) = (u32x2){pos[2], pos[3]};
    *(u32x2*)(rowp + ((2 ^ sw) * 32)) = (u32x2){neg[0], neg[1]};
    *(u32x2*)(rowp + ((3 ^ sw) * 32)) = (u32x2){neg[2], neg[3]};
  }
  STAMP(3);
  {
#pragma unroll
    for (int ct = 0; ct < 2; ++ct) {
      int d = ct == 0 ? l15 : (l15 < 8 ? 16 + l15 : 12 + l15);
      int G = (d * 37) >> 8, e = d - 7 * G;
      int rho = e < 4 ? 4 * G + e : 12 + 4 * G + e;
      f32x4 v = av[ct];
      if (ct == 1 && (l15 >> 2) == 2) { rho = pp == 0 ? 27 : (pp == 1 ? 19 : (pp == 2 ? 23 : 31)); if (pp == 0) v = (f32x4){1.f, 1.f, 1.f, 1.f}; }
      *(u32x2*)(vt + rho * 256 + (((2 * w + (g >> 1)) ^ (rho & 15)) * 16) + (g & 1) * 8) = (u32x2){pk2(v[0], v[1]), pk2(v[2], v[3])};
    }
  }
  waitv<0>();
  bar_l();
  STAMP(4);
  if (isg) {
    const float gate = rbf(__builtin_amdgcn_rcpf(1.f + __expf(-rbf(ga[0]))));
    if (lane < 16) st4c((__float_as_uint(gate) >> 16) | (t16 << 16), rWS, (tok0 + lane) * 4, WS_GATE);
  }
  if (w < 4) {
    const int sg = w >> 1, dt = w & 1;
    f32x4 t0 = {0, 0, 0, 0}, t1 = t0;
#pragma unroll
    for (int ks = 0; ks < 4; ++ks) {
      u32x4 af[2];
#pragma unroll
      for (int ft = 0; ft < 2; ++ft) {
        u32x2 a[2];
#pragma unroll
        for (int t = 0; t < 2; ++t) {
          int row = 32 * ks + 8 * g + 4 * t + q4;
          int sw = ((row >> 1) & 1) | (((row >> 3) & 1) << 1);
          a[t] = trread(phk, row * 128 + (((2 * sg + ft) ^ sw) * 32) + pp * 8);
        }
        af[ft] = (u32x4){a[0][0], a[0][1], a[1][0], a[1][1]};
      }
      u32x4 bf = *(const u32x4*)(vt + (16 * dt + l15) * 256 + (((4 * ks + g) ^ l15) * 16));
      t0 = mfma16(af[0], bf, t0); t1 = mfma16(af[1], bf, t1);
    }
    u32x4 un = {pk2(t0[0], t0[1]), pk2(t0[2], t0[3]), pk2(t1[0], t1[1]), (pk2(t1[2], t1[2]) & 0xffffu) | (t16 << 16)};
    st16c(un, rWS, (((h * 16 + tile) * 4 + w) * 64 + lane) * 16, WS_KVP);
  }
  STAMP(5);
  u32x4 pqp, pqn;
  u32x4 pv[16];
  const u32 pvo = ((h * 16 * 4 + (w & 3)) * 64 + lane) * 16;
  const u32 plo = WS_KVP + (((h * 16 + l15) * 4 + (w & 3)) * 64 + 63) * 16 + 12;
  u32 pl = 0;
  bool got = false;
  auto loadPV = [&]() {
#pragma unroll
    for (int t = 0; t < 16; ++t) pv[t] = LD1(rWS, pvo, WS_KVP + ((t + (tile & 3) * 4) & 15) * 4096);
  };
  {
    f32x4 aq[2] = {{0, 0, 0, 0}, {0, 0, 0, 0}};
#pragma unroll
    for (int i = 0; i < 7; ++i) {
      const char* qb = lds + (i == 6 ? A_Q6 : A_W + ((7 + (i >> 1)) % 5) * 8192 + (i & 1) * 4096);
      if (i == 4 && w >= 4) pl = ld4c(rWS, plo, 0);
#pragma unroll
      for (int ks = 0; ks < 2; ++ks)
#pragma unroll
        for (int ct = 0; ct < 2; ++ct) {
          u32x2 a = trread(qb, woff[ct] + ks * 2048);
          u32x2 b = trread(qb, woff[ct] + ks * 2048 + 256);
          aq[ct] = mfma16((u32x4){a[0], a[1], b[0], b[1]}, xf[2 * i + ks], aq[ct]);
        }
    }
    const u32x4 wq0 = *(const u32x4*)(lds + A_WPHI + lane * 16), wq1 = *(const u32x4*)(lds + A_WPHI + 1024 + lane * 16);
    u32x4 qb = {pk2(aq[0][0], aq[0][1]), pk2(aq[0][2], aq[0][3]), pk2(aq[1][0], aq[1][1]), pk2(aq[1][2], aq[1][3])};
    f32x4 z4 = {0, 0, 0, 0}; f32x4 pq[2];
    pq[0] = mfma16(wq0, qb, z4); pq[1] = mfma16(wq1, qb, z4);
    __builtin_amdgcn_sched_barrier(0);
    if (w >= 4) {
      got = __all((pl >> 16) == t16);
      if (got) loadPV(); else pl = ld4c(rWS, plo, 0);
    }
    __builtin_amdgcn_sched_barrier(0);
    dual_softmax(pq, pqp, pqn);
    __builtin_amdgcn_sched_barrier(0);
  }
  STAMP(6);
  const bool fin = j < 28;
  const int tt = 4 * x8 + j / 7, cg = j % 7, c0 = cg * 64;
  const int rt = w >> 1, kh = w & 1;
  const int ftok = tt * 64 + rt * 16 + l15;
  if (w >= 4) {
    if (!got) {
#pragma unroll 1
      for (int it = 0; it < MAXSPIN; ++it) {
        if (__all((pl >> 16) == t16)) break;
        __builtin_amdgcn_s_sleep(1);
        pl = ld4c(rWS, plo, 0);
      }
      loadPV();
    }
    u32 pend = 15;
#pragma unroll 1
    for (int it = 0; it < MAXSPIN; ++it) {
#pragma unroll
      for (int gq = 0; gq < 4; ++gq)
        if (pend & (1u << gq)) {
          const u32 m = (pv[4 * gq][3] ^ (t16 << 16)) | (pv[4 * gq + 1][3] ^ (t16 << 16)) | (pv[4 * gq + 2][3] ^ (t16 << 16)) | (pv[4 * gq + 3][3] ^ (t16 << 16));
          if (__all(m < 0x10000u)) pend &= ~(1u << gq);
        }
      if (!pend) break;
      __builtin_amdgcn_s_sleep(2);
#pragma unroll
      for (int gq = 0; gq < 4; ++gq)
        if (pend & (1u << gq)) {
#pragma unroll
          for (int t = 0; t < 4; ++t) pv[4 * gq + t] = ld16c(rWS, pvo, WS_KVP + ((4 * gq + t + (tile & 3) * 4) & 15) * 4096);
        }
    }
    float s[7] = {0, 0, 0, 0, 0, 0, 0};
    u32 selo = 0x00003f80u, sehi = 0x3f800000u;
    asm volatile("" : "+s"(selo), "+s"(sehi));
#pragma unroll
    for (int t = 0; t < 16; ++t) {
      s[0] = acc_sel(pv[t][0], selo, s[0]); s[1] = acc_sel(pv[t][0], sehi, s[1]); s[2] = acc_sel(pv[t][1], selo, s[2]); s[3] = acc_sel(pv[t][1], sehi, s[3]);
      s[4] = acc_sel(pv[t][2], selo, s[4]); s[5] = acc_sel(pv[t][2], sehi, s[5]); s[6] += bflo(pv[t][3]);
    }
    *(u32x4*)(lds + A_KVB + (w & 3) * 1024 + lane * 16) = (u32x4){pk2(s[0], s[1]), pk2(s[2], s[3]), pk2(s[4], s[5]), pk2(s[6], 0.f)};
  }
  bar_l();
  STAMP(7);
  {
    f32x4 o[2];
#pragma unroll
    for (int dt = 0; dt < 2; ++dt) {
      const u32x4 kp = *(const u32x4*)(lds + A_KVB + dt * 1024 + lane * 16), kn = *(const u32x4*)(lds + A_KVB + (2 + dt) * 1024 + lane * 16);
      o[dt] = mfma16(kp, pqp, (f32x4){0, 0, 0, 0});
      o[dt] = mfma16(kn, pqn, o[dt]);
    }
    u32 du = __float_as_uint(o[1][3]);
    auto r1 = __builtin_amdgcn_permlane32_swap(du, du, false, false);
    u32 d1 = r1[1];
    auto r2 = __builtin_amdgcn_permlane16_swap(d1, d1, false, false);
    float den = __uint_as_float(r2[0]);
    den = rbf(rbf(den) + 1e-6f);
    const float rd = __builtin_amdgcn_rcpf(den);
    const u32 n0 = pk2(o[0][0], o[0][1]), n1 = pk2(o[0][2], o[0][3]), n2 = pk2(o[1][0], o[1][1]), n3 = pk2(o[1][2], o[1][2]);
    u32x4 of = {pk2(bflo(n0) * rd, bfhi(n0) * rd), pk2(bflo(n1) * rd, bfhi(n1) * rd), pk2(bflo(n2) * rd, bfhi(n2) * rd), (pk2(bflo(n3) * rd, 0.f) & 0xffffu) | (t16 << 16)};
    const int rt16 = tile * 8 + w;
    st16c(of, rWS, ((rt16 * 16 + h) * 64 + lane) * 16, WS_OF);
  }
  STAMP(8);
  if (fin) {
    const int rt16 = tt * 4 + rt;
    {
      __builtin_amdgcn_sched_barrier(0);
      const u32x4 rWO = mkrs4(p.in[3], D * D * 2);
      const int jr = lane >> 3;
      const u32 vo = (jr < 7 ? jr : 6) * (D * 2) + cg * 128 + (((lane & 7) ^ (2 * (((jr >> 1) & 1) | ((w & 1) << 1)))) * 16);
#pragma unroll
      for (int m = 0; m < 8; ++m) adma16(rWO, lb + (w + 8 * m) * 1024, vo, (w + 8 * m) * (7 * D * 2));
      waitv<0>();
      BAR();
      CFENCE();
    }
    u32 wa[4];
    { const int s = 2 * (((q4 >> 1) & 1) | ((g & 1) << 1));
#pragma unroll
      for (int ct = 0; ct < 4; ++ct) wa[ct] = kh * 32768 + (8 * g + q4) * 128 + (((2 * ct) ^ s) * 16) + pp * 8; }
    u32x4 wf[NPRE][4];
    auto readWO = [&](int hh, u32x4 (&f)[4]) {
#pragma unroll
      for (int ct = 0; ct < 4; ++ct) {
        u32x2 a = trread(lds, wa[ct] + hh * 4096);
        u32x2 b = trread(lds, wa[ct] + hh * 4096 + 512);
        f[ct] = (u32x4){a[0], a[1], b[0], b[1]};
      }
    };
#pragma unroll
    for (int hh = 0; hh < NPRE; ++hh) readWO(hh, wf[hh]);
    __builtin_amdgcn_sched_barrier(0);
    u32x4 of[8];
    u32 yv[2][4], gv;
    const u32 ovo = ((rt16 * 16 + 8 * kh) * 64 + lane) * 16;
    const u32 yvo = (c0 + 32 * kh + 4 * g) * (S * 4) + ftok * 4;
    const u32 tw = t16 << 16;
    {
      const u32 po = lane < 8 ? WS_OF + ((rt16 * 16 + 8 * kh + lane) * 64 + 63) * 16 + 12 : WS_GATE + (rt16 * 16 + 15) * 4;
#pragma unroll 1
      for (int it = 0; it < MAXSPIN; ++it) {
        const u32 v = ld4c(rWS, po, 0);
        if (__all((v >> 16) == t16)) break;
        __builtin_amdgcn_s_sleep(P2SLEEP);
      }
    }
    DBGCNT(st_[12] = (u32)__builtin_amdgcn_s_memrealtime();)
#pragma unroll
    for (int t = 0; t < 8; ++t) of[t] = LD2(rWS, ovo, WS_OF + t * 1024);
    gv = ld4c(rWS, ftok * 4, WS_GATE);
    auto loadY = [&]() {
#pragma unroll
      for (int m = 0; m < 2; ++m)
#pragma unroll
        for (int i = 0; i < 4; ++i) yv[m][i] = ld4c(rWS, yvo, WS_YT + (16 * m + i) * (S * 4));
    };
    loadY();
#pragma unroll 1
    for (int it = 0; it < MAXSPIN; ++it) {
      u32 m = gv ^ tw;
#pragma unroll
      for (int t = 0; t < 8; ++t) m |= of[t][3] ^ tw;
      if (__all(m < 0x10000u)) break;
      __builtin_amdgcn_s_sleep(2);
#pragma unroll
      for (int t = 0; t < 8; ++t) of[t] = ld16c(rWS, ovo, WS_OF + t * 1024);
      gv = ld4c(rWS, ftok * 4, WS_GATE);
    }
    STAMP(9);
    f32x4 acc[4];
#pragma unroll
    for (int ct = 0; ct < 4; ++ct) acc[ct] = (f32x4){0, 0, 0, 0};
#pragma unroll
    for (int hh = 0; hh < 8; ++hh) {
      u32x4 ob = of[hh]; ob[3] &= 0xffffu;
#pragma unroll
      for (int ct = 0; ct < 4; ++ct) acc[ct] = mfma16(wf[hh % NPRE][ct], ob, acc[ct]);
      if (hh + NPRE < 8) readWO(hh + NPRE, wf[hh % NPRE]);
      __builtin_amdgcn_sched_barrier(0);
    }
    STAMP(10);
    const f32x4 c0_ = acc[0], c1_ = acc[1], c2_ = acc[2], c3_ = acc[3];
    {
      f32x4* rw = (f32x4*)(lds + A_RED + w * 2048) + lane;
      rw[0] = kh ? c0_ : c2_; rw[64] = kh ? c1_ : c3_;
    }
    const f32x4 keep0 = kh ? c2_ : c0_, keep1 = kh ? c3_ : c1_;
    bar_l();
#pragma unroll 1
    for (int it = 0; it < MAXSPIN; ++it) {
      const u32 m = (yv[0][0] ^ tw) | (yv[0][1] ^ tw) | (yv[0][2] ^ tw) | (yv[0][3] ^ tw) | (yv[1][0] ^ tw) | (yv[1][1] ^ tw) | (yv[1][2] ^ tw) | (yv[1][3] ^ tw);
      if (__all(m < 0x10000u)) break;
      __builtin_amdgcn_s_sleep(4);
      loadY();
    }
    DBGCNT(st_[3] = (u32)__builtin_amdgcn_s_memrealtime();)
    const f32x4* rr = (const f32x4*)(lds + A_RED + (w ^ 1) * 2048) + lane;
    const float gg = bflo(gv), og = rbf(1.f - gg);
    u32 res[2][2];
#pragma unroll
    for (int m = 0; m < 2; ++m) {
      f32x4 a = rr[m * 64];
      a += m ? keep1 : keep0;
      float r[4];
#pragma unroll
      for (int i = 0; i < 4; ++i) {
        float av_ = rbf(a[i]);
        r[i] = rbf(smul(gg, av_)) + rbf(smul(og, bflo(yv[m][i])));
      }
      res[m][0] = pk2(r[0], r[1]); res[m][1] = pk2(r[2], r[3]);
    }
    u32x4 ov;
    {
      auto s0 = __builtin_amdgcn_permlane32_swap(res[0][0], res[1][0], false, false);
      auto s1 = __builtin_amdgcn_permlane32_swap(res[0][1], res[1][1], false, false);
      auto t0 = __builtin_amdgcn_permlane16_swap(s0[0], s0[1], false, false);
      auto t1 = __builtin_amdgcn_permlane16_swap(s1[0], s1[1], false, false);
      ov = (u32x4){t0[0], t1[0], t0[1], t1[1]};
    }
    *(u32x4*)(p.out + (size_t)ftok * D + c0 + 32 * kh + 8 * g) = ov;
    STAMP(11);
  }
  else if (x8 == 0 && j == 31) {
    if (tid < 256) {
      const u32 off = tid < 224 ? WS_YT + 2 * tid * (S * 4) : WS_OF + ((tid - 224) & 15) * 1024 + 12;
#pragma unroll 1
      for (int it = 0; it < MAXSPIN; ++it) {
        const u32 v = ld4c(rWS, off, 0);
        if (__all((v >> 16) == t16)) break;
        __builtin_amdgcn_s_sleep(16);
      }
    }
    CFENCE();
    BAR();
    if (tid == 0) __hip_atomic_store((u32*)(ws + WS_GEN), tag, __ATOMIC_RELAXED, __HIP_MEMORY_SCOPE_AGENT);
  }
  LFLUSH(ws);
  STAMP_FLUSH(ws);
}

DI f32x2 cmul(f32x2 a, f32x2 b) {
  float t0, t1, re, im;
  asm("v_mul_f32 %0, %1, %2" : "=v"(t0) : "v"(a.y), "v"(b.y));
  asm("v_mul_f32 %0, %1, %2" : "=v"(t1) : "v"(a.y), "v"(b.x));
  asm("v_fma_f32 %0, %1, %2, -%3" : "=v"(re) : "v"(a.x), "v"(b.x), "v"(t0));
  asm("v_fma_f32 %0, %1, %2, %3" : "=v"(im) : "v"(a.x), "v"(b.y), "v"(t1));
  return (f32x2){re, im};
}
DI f32x2 cexp_pow(float mr, float pr, float a) {
  float m = __builtin_amdgcn_exp2f(mr * a);
  float t = __builtin_amdgcn_fractf(pr * a);
  return (f32x2){m * __builtin_amdgcn_cosf(t), m * __builtin_amdgcn_sinf(t)};
}
constexpr int KG_ROW = 272, KG_SLAB = 16 * KG_ROW;
constexpr int B_UC = 34816, UCH = 16656, B_KR = 68128, B_KTAB = 77344, B_DUM = 80416, B_TIMG = B_KR, B_RED = 0;
constexpr int UC0 = 8, UC1 = 4176, UC2 = 8336, UC3 = 12496;

DI void roleB(const Params& p, int x8, int jb, char* lds) {
  const int tid = threadIdx.x, lane = tid & 63, w = __builtin_amdgcn_readfirstlane(tid >> 6);
  const int g = lane >> 4, l15 = lane & 15;
  char* ws = p.ws;
  STAMP_DECL
  STAMP(0);
  __builtin_amdgcn_s_setprio(3);
  const rsrc_t rWS = mkrs(ws, WS_END);
  const int tt = 4 * x8 + jb / 7, cg = jb % 7, ch0 = 2 * (x8 * 28 + jb);
  const u32 gen = sload(ws + WS_GEN);
  if (w == 7) {
    const u32x4 rc = mkrs4((const void*)(__builtin_amdgcn_s_getpc() & ~(unsigned long long)127), CODE_BYTES);
#pragma unroll
    for (int i = 0; i < (CODE_BYTES + 8191) / 8192; ++i) atouch4(rc, ldsaddr(lds) + B_DUM, lane * 128, i * 8192);
  } else if (w >= 5) {
    atouch4(mkrs4(p.in[3], D * D * 2), ldsaddr(lds) + B_DUM, lane * 128, (jb * 2 + (w - 5)) * 7168);
  }
  const u32x4 xv = *(const u32x4*)(p.in[0] + (size_t)(tt * 64 + (tid >> 3)) * D + cg * 64 + (tid & 7) * 8);
  const int d = ch0 + (w >> 2);
  const int mo = d * NM + (w & 3) * 64 + lane;
  const u32 are = p.in[5][mo], aim = p.in[6][mo], bre = p.in[7][mo], bim = p.in[8][mo], cre = p.in[9][mo], cim = p.in[10][mo];
  const u32 ldt = p.in[11][d], dskip = p.in[12][d];
  __builtin_amdgcn_sched_barrier(0);
  const u32 tag = gen + 1u, tw = (tag & 0xffffu) << 16;
  {
    char* img = lds + B_TIMG;
    u32* dst = (u32*)(img + (tid >> 3) * 132 + (tid & 7) * 16);
    dst[0] = xv[0]; dst[1] = xv[1]; dst[2] = xv[2]; dst[3] = xv[3];
    bar_l();
    const int chl = tid >> 3, oc = tid & 7;
    u32 o[8];
#pragma unroll
    for (int jj = 0; jj < 8; ++jj) o[jj] = *(const u16*)(img + (4 * oc + (jj & 3) + 32 * (jj >> 2)) * 132 + chl * 2) | tw;
    st16c((u32x4){o[0], o[1], o[2], o[3]}, rWS, (cg * 64 + chl) * (S * 4) + tt * 256 + oc * 16, WS_XT);
    st16c((u32x4){o[4], o[5], o[6], o[7]}, rWS, (cg * 64 + chl) * (S * 4) + tt * 256 + oc * 16 + 128, WS_XT);
  }
  __builtin_amdgcn_s_setprio(BPRIO);
  STAMP(1);
  char* slab = lds + w * KG_SLAB;
  const int cch = tid >> 8, cq = tid & 255;
  u32x4 ua, ub, na, nb;
  const u32 uvo = (ch0 + cch) * (S * 4) + cq * 16;
  auto loadU = [&]() {
    ua = ld16c(rWS, uvo, WS_XT); ub = ld16c(rWS, uvo + 4096, WS_XT);
    na = ua; nb = ub;
    if (lane == 63) { na = ld16c(rWS, uvo + 16, WS_XT); nb = ld16c(rWS, cq < 255 ? uvo + 4096 + 16 : uvo, WS_XT); }
  };
  f32x4 acc[3];
  {
    const float dt = rbf(__expf(bflo(ldt)));
    const float ar = dt * bflo(are), ai = dt * bflo(aim), br = dt * bflo(bre), bi = dt * bflo(bim), cr = bflo(cre), ci = bflo(cim);
    const float mr = ar * 1.4426950408889634f, pr = ai * 0.15915494309189535f;
    f32x2 W = {cr * br - ci * bi, -(cr * bi + ci * br)};
    f32x2 z1c = cexp_pow(mr, -pr, 1.f);
    f32x2 z8 = cexp_pow(mr, pr, 8.f);
    f32x2 a128 = cexp_pow(mr, -pr, 128.f);
    asm volatile("s_mov_b32 m0, %0" ::"s"(ldsaddr(slab)) : "memory");
#define WROWT(r, v) do { const f32x2 v_ = (v); asm volatile("ds_write_addtid_b32 %0 offset:%1" ::"v"(pk2(v_.x, v_.y)), "i"((r) * KG_ROW) : "memory"); } while (0)
    const char* rd = slab + l15 * KG_ROW + g * 16;
    u32x4 yf[4];
    {
      f32x2 y[16];
      y[1] = z8;
      f32x2 y1c = z8; asm volatile("" : "+v"(y1c));
      y[2] = cmul(y[1], y1c); y[3] = cmul(y[2], y[1]); y[4] = cmul(y[3], y[1]);
      y[5] = cmul(y[4], y[1]); y[6] = cmul(y[4], y[2]); y[7] = cmul(y[4], y[3]); y[8] = cmul(y[5], y[3]);
#pragma unroll
      for (int c = 9; c < 16; ++c) y[c] = cmul(y[8], y[c - 8]);
      WROWT(0, ((f32x2){1.f, 0.f}));
#pragma unroll
      for (int c = 1; c < 16; ++c) WROWT(c, y[c]);
    }
#pragma unroll
    for (int ks = 0; ks < 4; ++ks) yf[ks] = *(const u32x4*)(rd + ks * 64);
    CFENCE();
    f32x2 R0[8];
    {
      f32x2 zc = z1c; asm volatile("" : "+v"(zc));
      f32x2 z2 = cmul(z1c, zc);
      f32x2 z2c = z2; asm volatile("" : "+v"(z2c));
      f32x2 z4 = cmul(z2, z2c);
      R0[0] = W; R0[1] = cmul(W, z1c); R0[2] = cmul(W, z2); R0[3] = cmul(R0[1], z2);
#pragma unroll
      for (int b = 0; b < 4; ++b) R0[4 + b] = cmul(R0[b], z4);
    }
    f32x2 A[17];
    {
      A[1] = a128;
      f32x2 a1c = a128; asm volatile("" : "+v"(a1c));
      A[2] = cmul(A[1], a1c); A[3] = cmul(A[2], A[1]); A[4] = cmul(A[3], A[1]);
      A[5] = cmul(A[4], A[1]); A[6] = cmul(A[4], A[2]); A[7] = cmul(A[4], A[3]); A[8] = cmul(A[5], A[3]);
#pragma unroll
      for (int m = 9; m < 16; ++m) A[m] = cmul(A[8], A[m - 8]);
      A[16] = cmul(A[9], A[7]);
    }
    {
      f32x2 t[8];
#pragma unroll
      for (int b = 0; b < 8; ++b) t[b] = cmul(R0[b], A[1]);
#pragma unroll
      for (int b = 0; b < 8; ++b) { WROWT(b, R0[b]); WROWT(8 + b, t[b]); }
    }
    acc[0] = (f32x4){0, 0, 0, 0};
#pragma unroll
    for (int ks = 0; ks < 4; ++ks) { u32x4 xf = *(const u32x4*)(rd + ks * 64); acc[0] = mfma16(xf, yf[ks], acc[0]); }
    CFENCE();
    STAMP(7);
    {
      f32x2 t[16];
#pragma unroll
      for (int hh = 0; hh < 2; ++hh)
#pragma unroll
        for (int b = 0; b < 4; ++b) t[b + 4 * hh] = cmul(R0[2 * b], A[2 + hh]);
#pragma unroll
      for (int hh = 0; hh < 4; ++hh)
#pragma unroll
        for (int b = 0; b < 2; ++b) t[8 + b + 2 * hh] = cmul(R0[4 * b], A[4 + hh]);
#pragma unroll
      for (int r = 0; r < 16; ++r) WROWT(r, t[r]);
    }
    acc[1] = (f32x4){0, 0, 0, 0};
#pragma unroll
    for (int ks = 0; ks < 4; ++ks) { u32x4 xf = *(const u32x4*)(rd + ks * 64); acc[1] = mfma16(xf, yf[ks], acc[1]); }
    CFENCE();
    STAMP(9);
    __builtin_amdgcn_s_setprio(3);
    loadU();
    __builtin_amdgcn_s_setprio(BPRIO);
    __builtin_amdgcn_sched_barrier(0);
    {
      f32x2 t[9];
#pragma unroll
      for (int hh = 0; hh < 9; ++hh) t[hh] = cmul(W, A[8 + hh]);
#pragma unroll
      for (int hh = 0; hh < 9; ++hh) WROWT(hh, t[hh]);
    }
    acc[2] = (f32x4){0, 0, 0, 0};
#pragma unroll
    for (int ks = 0; ks < 4; ++ks) { u32x4 xf = *(const u32x4*)(rd + ks * 64); acc[2] = mfma16(xf, yf[ks], acc[2]); }
    CFENCE();
    float* kt = (float*)slab;
    *(f32x4*)(kt + 8 * l15 + 4 * (g & 1) + 128 * (g >> 1)) = acc[0];
    {
      const int t0 = g < 2 ? 256 + 4 * l15 + 64 * g : 384 + 2 * l15 + 64 * (g - 2);
      const int t1 = g < 2 ? t0 + 2 : t0 + 32;
      *(f32x2*)(kt + t0) = (f32x2){acc[1][0], acc[1][1]};
      *(f32x2*)(kt + t1) = (f32x2){acc[1][2], acc[1][3]};
    }
#pragma unroll
    for (int i = 0; i < 4; ++i) kt[512 + l15 + 16 * (4 * g + i)] = acc[2][i];
  }
  STAMP(2);
  {
    __builtin_amdgcn_s_setprio(3);
#pragma unroll 1
    for (int it = 0; it < MAXSPIN; ++it) {
      const u32 m = (ua[0] ^ tw) | (ua[1] ^ tw) | (ua[2] ^ tw) | (ua[3] ^ tw) | (ub[0] ^ tw) | (ub[1] ^ tw) | (ub[2] ^ tw) | (ub[3] ^ tw) | (na[0] ^ tw) | (na[1] ^ tw) | (na[2] ^ tw) | (nb[0] ^ tw) | (nb[1] ^ tw) | (nb[2] ^ tw);
      if (__all(m < 0x10000u)) break;
      __builtin_amdgcn_s_sleep(2);
      loadU();
    }
    __builtin_amdgcn_s_setprio(BPRIO);
    STAMP(10);
    char* base = lds + B_UC + cch * UCH + (16 + cq * 4) * 2;
    u32 e00, sh00;
#pragma unroll
    for (int hf = 0; hf < 2; ++hf) {
      const u32x4 uu = hf ? ub : ua, nn = hf ? nb : na;
      const u32 e0 = __builtin_amdgcn_perm(uu[1], uu[0], 0x05040100u), e1 = __builtin_amdgcn_perm(uu[3], uu[2], 0x05040100u);
      u32 f0 = __builtin_amdgcn_perm(nn[1], nn[0], 0x05040100u), f1 = nn[2] & 0xffffu;
      if (hf == 1 && cq == 255) { f0 = 0; f1 = 0; }
      const u32 n0 = __builtin_amdgcn_update_dpp(f0, e0, 0x130, 0xf, 0xf, false), n1 = __builtin_amdgcn_update_dpp(f1, e1, 0x130, 0xf, 0xf, false);
      const u32 s0 = __builtin_amdgcn_alignbit(e1, e0, 16), s1 = __builtin_amdgcn_alignbit(n0, e1, 16), s2 = __builtin_amdgcn_alignbit(n1, n0, 16);
      char* bh = base + hf * 2048;
      *(u32x2*)(bh + UC0) = (u32x2){e0, e1};
      *(u32x2*)(bh + UC1) = (u32x2){s0, s1};
      *(u32x2*)(bh + UC2) = (u32x2){e1, n0};
      *(u32x2*)(bh + UC3) = (u32x2){s1, s2};
      if (hf == 0) { e00 = e0; sh00 = s0; }
    }
    if (cq == 0) {
      char* b0 = lds + B_UC + cch * UCH;
      const u32x4 z = {0, 0, 0, 0};
      *(u32x2*)(b0 + UC0) = (u32x2){0, 0}; *(u32x2*)(b0 + UC0 + 8) = (u32x2){0, 0}; *(u32x2*)(b0 + UC0 + 16) = (u32x2){0, 0}; *(u32x2*)(b0 + UC0 + 24) = (u32x2){0, 0};
      *(u32x4*)(b0 + UC1) = z; *(u32x4*)(b0 + UC1 + 16) = (u32x4){0, 0, 0, e00 << 16};
      *(u32x4*)(b0 + UC2) = z; *(u32x4*)(b0 + UC2 + 16) = (u32x4){0, 0, 0, e00};
      *(u32x4*)(b0 + UC3) = z; *(u32x4*)(b0 + UC3 + 16) = (u32x4){0, 0, e00 << 16, sh00};
    }
  }
  __builtin_amdgcn_s_setprio(BPRIO2);
  bar_l();
  STAMP(3);
  if (tid < 384) {
    const int ch = tid >= 192 ? 1 : 0, t4 = (tid - 192 * ch) * 4;
    const float* k0 = (const float*)(lds + 4 * ch * KG_SLAB) + t4;
    f32x4 s = *(const f32x4*)k0;
#pragma unroll
    for (int q = 1; q < 4; ++q) s += *(const f32x4*)(k0 + q * (KG_SLAB / 4));
    *(u32x2*)(lds + B_KTAB + ch * 1536 + t4 * 2) = (u32x2){pk2(s[0], s[1]), pk2(s[2], s[3])};
  }
  bar_l();
  {
    const int jhi = 16 * (cq >> 1) + 15 - (cq & 1) * 8;
    const float fj = (float)jhi;
    const char* kt = lds + B_KTAB + cch * 1536;
    u32 o[4];
#pragma unroll
    for (int e = 0; e < 4; ++e) {
      u32 bits = pk2(fj - (float)(2 * e), fj - (float)(2 * e + 1));
      u32 t0 = (bits & 0xffffu) - 0x4280u, t1 = (bits >> 16) - 0x4280u;
      if (jhi < 256) { t0 = jhi - 2 * e; t1 = jhi - 2 * e - 1; }
      u32 lo = *(const u16*)(kt + t0 * 2), hi = *(const u16*)(kt + t1 * 2);
      o[e] = lo | (hi << 16);
    }
    char* kr = lds + B_KR + cch * 4608;
    *(u32x4*)(kr + 512 + cq * 16) = (u32x4){o[0], o[1], o[2], o[3]};
    if (cq < 32) *(u32x4*)(kr + cq * 16) = (u32x4){0, 0, 0, 0};
  }
  bar_l();
  STAMP(4);
  {
    const int ch = w >> 2, rh = (w >> 1) & 1, sh = w & 1;
    const int idx0 = 8 * g + l15 + 1, cc = idx0 & 3;
    const u32 ucb = cc == 0 ? UC0 : (cc == 1 ? UC1 : (cc == 2 ? UC2 : UC3));
    const LAS char* abase = (const LAS char*)(lds + B_UC + ch * UCH + ucb + (idx0 & ~3) * 2 + rh * 256);
    const char* bbase = lds + B_KR + ch * 4608 + (256 + 16 * (l15 - (g >> 1)) + 8 * (g & 1)) * 2 - rh * 256;
    f32x4 c4[4];
#pragma unroll
    for (int i = 0; i < 4; ++i) c4[i] = (f32x4){0, 0, 0, 0};
    if (sh == 0) {
#pragma unroll 1
      for (int rr = 0; rr < 4; ++rr) {
        u32x4 af[8], bf[8];
#pragma unroll
        for (int a = 0; a < 8; ++a) {
          const LAS char* pa = abase + (256 * a) * 2 + rr * 64;
          u32x2 lo = *(const volatile LAS u32x2*)pa, hi = *(const volatile LAS u32x2*)(pa + 8);
          af[a] = (u32x4){lo[0], lo[1], hi[0], hi[1]};
          bf[a] = *(const u32x4*)(bbase + (256 * a) * 2 - rr * 64);
        }
        c4[0] = mfma16(af[0], bf[0], c4[0]);
        c4[1] = mfma16(af[0], bf[1], c4[1]); c4[1] = mfma16(af[1], bf[0], c4[1]);
#pragma unroll
        for (int a = 0; a < 8; ++a) {
          if (a < 7) c4[2] = mfma16(af[a], bf[6 - a], c4[2]);
          c4[3] = mfma16(af[a], bf[7 - a], c4[3]);
        }
      }
    } else {
#pragma unroll 1
      for (int rr = 0; rr < 4; ++rr) {
        u32x4 af[6], bf[6];
#pragma unroll
        for (int a = 0; a < 6; ++a) {
          const LAS char* pa = abase + (256 * a) * 2 + rr * 64;
          u32x2 lo = *(const volatile LAS u32x2*)pa, hi = *(const volatile LAS u32x2*)(pa + 8);
          af[a] = (u32x4){lo[0], lo[1], hi[0], hi[1]};
          bf[a] = *(const u32x4*)(bbase + (256 * a) * 2 - rr * 64);
        }
#pragma unroll
        for (int a = 0; a < 6; ++a) {
          if (a < 3) c4[0] = mfma16(af[a], bf[2 - a], c4[0]);
          if (a < 4) c4[1] = mfma16(af[a], bf[3 - a], c4[1]);
          if (a < 5) c4[2] = mfma16(af[a], bf[4 - a], c4[2]);
          c4[3] = mfma16(af[a], bf[5 - a], c4[3]);
        }
      }
    }
    STAMP(5);
    const f32x4 e0 = c4[0], e1 = c4[1], e2 = c4[2], e3 = c4[3];
    f32x4* xw = (f32x4*)(lds + B_RED + w * 2048) + lane;
    xw[0] = rh ? e0 : e2; xw[64] = rh ? e1 : e3;
    bar_l();
    const f32x4* xr = (const f32x4*)(lds + B_RED + (w ^ 2) * 2048) + lane;
    f32x4 v0 = xr[0], v1 = xr[64];
    v0 += rh ? e2 : e0; v1 += rh ? e3 : e1;
    const int pt = sh == 0 ? (rh ? 6 : 0) : (rh ? 4 : 2);
    char* strip = lds + B_RED + 16384 + w * 1024;
    *(u32x2*)(strip + (4 * l15 + g) * 8) = (u32x2){pk2(v0[0], v0[1]), pk2(v0[2], v0[3])};
    *(u32x2*)(strip + 512 + (4 * l15 + g) * 8) = (u32x2){pk2(v1[0], v1[1]), pk2(v1[2], v1[3])};
    const char* ub0 = lds + B_UC + ch * UCH + UC0 + (16 + 256 * pt + 4 * lane) * 2;
    const u32x2 x0 = *(const u32x2*)ub0, x1 = *(const u32x2*)(ub0 + 512);
    const float dsk = bflo(dskip);
    CFENCE();
    WAIT_L(0);
    const u32x2 y0 = *(const u32x2*)(strip + lane * 8), y1 = *(const u32x2*)(strip + 512 + lane * 8);
    __builtin_amdgcn_s_setprio(3);
    const u32 yo = (ch0 + ch) * (S * 4) + (256 * pt + 4 * lane) * 4;
#pragma unroll
    for (int hf = 0; hf < 2; ++hf) {
      const u32x2 xx = hf ? x1 : x0, yy = hf ? y1 : y0;
      const u32 pa = pk2(smul(bflo(xx[0]), dsk), smul(bfhi(xx[0]), dsk)), pb = pk2(smul(bflo(xx[1]), dsk), smul(bfhi(xx[1]), dsk));
      const u32 ra = pk2(bflo(yy[0]) + bflo(pa), bfhi(yy[0]) + bfhi(pa)), rb = pk2(bflo(yy[1]) + bflo(pb), bfhi(yy[1]) + bfhi(pb));
      st16c((u32x4){(ra & 0xffffu) | tw, (ra >> 16) | tw, (rb & 0xffffu) | tw, (rb >> 16) | tw}, rWS, yo + hf * 1024, WS_YT);
    }
    STAMP(6);
  }
  STAMP_FLUSH(ws);
}

__global__ __launch_bounds__(512, 4) void fused(Params p) {
  __shared__ __attribute__((aligned(16))) char lds[LDS_BYTES];
  const int b = blockIdx.x, x8 = b & 7, j = b >> 3;
  if (j < 32) roleA(p, x8, j, lds);
  else roleB(p, x8, j - 32, lds);
}

extern "C" void kernel_launch(void* const* d_in, const int* in_sizes, int n_in, void* d_out, int out_size, void* d_ws, size_t ws_size, hipStream_t stream) {
  Params p;
  for (int i = 0; i < 14; ++i) p.in[i] = (const u16*)d_in[i];
  p.ws = (char*)d_ws; p.out = (u16*)d_out;
  fused<<<480, 512, 0, stream>>>(p);
}
```

```cpp
#define MULT 0
#include <hip/hip_runtime.h>
#include <stdint.h>

typedef unsigned short u16;
typedef unsigned int u32;
typedef short s16x4 __attribute__((ext_vector_type(4)));
typedef __bf16 bf16x8 __attribute__((ext_vector_type(8)));
typedef __bf16 bf16x2 __attribute__((ext_vector_type(2)));
typedef float f32x4 __attribute__((ext_vector_type(4)));
typedef float f32x2 __attribute__((ext_vector_type(2)));
typedef u32 u32x4 __attribute__((ext_vector_type(4)));
typedef u32 u32x2 __attribute__((ext_vector_type(2)));
typedef __amdgpu_buffer_rsrc_t rsrc_t;
#define LAS __attribute__((address_space(3)))
#define GAS __attribute__((address_space(1)))
#define DI __device__ __forceinline__

constexpr int S = 2048, D = 448, H = 16, DH = 28, NM = 256;

constexpr u32 WS_GEN = 0;
constexpr u32 WS_FLAG = 4096, FSTR = 128;
constexpr u32 F_B = 0;
constexpr u32 F_A = 224;
constexpr u32 NFLAG = F_A + 256;
constexpr u32 WS_XT = 1u << 20;
constexpr u32 WS_KVP = WS_XT + D * S * 4;
constexpr u32 WS_OF = WS_KVP + 16 * 16 * 4 * 1024;
constexpr u32 WS_YT = WS_OF + 128 * 16 * 1024;
constexpr u32 WS_GATE = WS_YT + D * S * 4;
constexpr u32 WS_END = WS_GATE + S * 4;
static_assert(WS_FLAG + NFLAG * FSTR <= WS_XT, "flags");
constexpr int MAXSPIN = 400000;
#define CODE_BYTES 8192

#define STAMP(i)
#define STAMP_DECL
#define STAMP_FLUSH(ws)

DI float bflo(u32 v) { return __uint_as_float(v << 16); }
DI float bfhi(u32 v) { return __uint_as_float(v & 0xffff0000u); }
DI u32 pk2(float lo, float hi) { f32x2 p = {lo, hi}; bf16x2 q = __builtin_convertvector(p, bf16x2); return __builtin_bit_cast(u32, q); }
DI float smul(float a, float b) { float r; asm("v_mul_f32 %0, %1, %2" : "=v"(r) : "v"(a), "v"(b)); return r; }
DI float rbf(float f) { return bflo(pk2(f, f)); }
DI f32x4 mfma16(u32x4 a, u32x4 b, f32x4 c) {
  return __builtin_amdgcn_mfma_f32_16x16x32_bf16(__builtin_bit_cast(bf16x8, a), __builtin_bit_cast(bf16x8, b), c, 0, 0, 0);
}
DI u32x2 trread(const char* lds_base, u32 off) {
  s16x4 t = __builtin_amdgcn_ds_read_tr16_b64_v4i16((LAS s16x4*)((LAS char*)lds_base + off));
  return __builtin_bit_cast(u32x2, t);
}
DI rsrc_t mkrs(const void* p, u32 bytes) { return __builtin_amdgcn_make_buffer_rsrc((void*)p, 0, bytes, 0x00020000); }
DI void dma16(rsrc_t r, char* lds_dst, u32 voff, u32 soff) { __builtin_amdgcn_raw_ptr_buffer_load_lds(r, (LAS void*)lds_dst, 16, voff, soff, 0, 0); }
DI void touch4(rsrc_t r, char* lds_dst, u32 voff, u32 soff) { __builtin_amdgcn_raw_ptr_buffer_load_lds(r, (LAS void*)lds_dst, 4, voff, soff, 0, 0); }
DI u32x4 mkrs4(const void* p, u32 bytes) { unsigned long long a = (unsigned long long)p; return (u32x4){(u32)a, (u32)(a >> 32) & 0xffffu, bytes, 0x00020000u}; }
DI u32 ldsaddr(const char* p) { return (u32)(uintptr_t)(LAS const char*)p; }
DI void adma16(u32x4 rs, u32 lds_dst, u32 voff, u32 soff) {
  asm volatile("s_mov_b32 m0, %0\n\ts_nop 0\n\tbuffer_load_dwordx4 %1, %2, %3 offen lds" ::"s"(lds_dst), "v"(voff), "s"(rs), "s"(soff) : "memory");
}
DI void adma16h(u32x4 rs, u32 lds_dst, u32 voff, u32 soff) {
  asm volatile("s_mov_b32 m0, %0\n\ts_mov_b32 exec_hi, 0\n\tbuffer_load_dwordx4 %1, %2, %3 offen lds\n\ts_mov_b32 exec_hi, -1" ::"s"(lds_dst), "v"(voff), "s"(rs), "s"(soff) : "memory");
}
DI u32 sload(const void* p) { return *(const __attribute__((address_space(4))) u32*)(unsigned long long)p; }
DI void atouch4(u32x4 rs, u32 lds_dst, u32 voff, u32 soff) {
  asm volatile("s_mov_b32 m0, %0\n\ts_nop 0\n\tbuffer_load_dword %1, %2, %3 offen lds" ::"s"(lds_dst), "v"(voff), "s"(rs), "s"(soff) : "memory");
}
DI u32x4 ald16(u32x4 rs, u32 voff, u32 soff) {
  u32x4 r; asm volatile("buffer_load_dwordx4 %0, %1, %2, %3 offen" : "=v"(r) : "v"(voff), "s"(rs), "s"(soff) : "memory"); return r;
}
DI void ald16io(u32x4& r, u32x4 rs, u32 voff, u32 soff) {
  asm volatile("buffer_load_dwordx4 %0, %1, %2, %3 offen" : "+v"(r) : "v"(voff), "s"(rs), "s"(soff) : "memory");
}
DI void ald16cio(u32x4& r, u32x4 rs, u32 voff, u32 soff) {
  asm volatile("buffer_load_dwordx4 %0, %1, %2, %3 offen sc1" : "+v"(r) : "v"(voff), "s"(rs), "s"(soff) : "memory");
}
DI void wait8(u32x4* a) { asm volatile("s_waitcnt vmcnt(0)" : "+v"(a[0]), "+v"(a[1]), "+v"(a[2]), "+v"(a[3]), "+v"(a[4]), "+v"(a[5]), "+v"(a[6]), "+v"(a[7])::"memory"); }
DI void undef8(u32x4* a) { asm volatile("" : "=v"(a[0]), "=v"(a[1]), "=v"(a[2]), "=v"(a[3]), "=v"(a[4]), "=v"(a[5]), "=v"(a[6]), "=v"(a[7])); }
template <int N> DI void waitv4(u32x4& a, u32x4& b, u32x4& c, u32x4& d) { asm volatile("s_waitcnt vmcnt(%4)" : "+v"(a), "+v"(b), "+v"(c), "+v"(d) : "i"(N) : "memory"); }
DI u32x4 ld16(rsrc_t r, u32 voff, u32 soff) { return __builtin_amdgcn_raw_buffer_load_b128(r, voff, soff, 0); }
DI u32x4 ld16c(rsrc_t r, u32 voff, u32 soff) { return __builtin_amdgcn_raw_buffer_load_b128(r, voff, soff, 16); }
DI u32x2 ld8c(rsrc_t r, u32 voff, u32 soff) { return __builtin_amdgcn_raw_buffer_load_b64(r, voff, soff, 16); }
DI u32 ld4c(rsrc_t r, u32 voff, u32 soff) { return __builtin_amdgcn_raw_buffer_load_b32(r, voff, soff, 16); }
DI u32 ld2c(rsrc_t r, u32 voff, u32 soff) { return __builtin_amdgcn_raw_buffer_load_b16(r, voff, soff, 16); }
DI void st16c(u32x4 v, rsrc_t r, u32 voff, u32 soff) { __builtin_amdgcn_raw_buffer_store_b128(v, r, voff, soff, 16); }
DI void st4c(u32 v, rsrc_t r, u32 voff, u32 soff) { __builtin_amdgcn_raw_buffer_store_b32(v, r, voff, soff, 16); }
#define WAIT_L(n) asm volatile("s_waitcnt lgkmcnt(" #n ")" ::: "memory")
#define BAR() __builtin_amdgcn_s_barrier()
#define CFENCE() asm volatile("" ::: "memory")
template <int N> DI void waitv() { asm volatile("s_waitcnt vmcnt(%0)" ::"i"(N) : "memory"); }
DI void bar_l() { WAIT_L(0); BAR(); CFENCE(); }

DI u32 flag_off(u32 idx) { return WS_FLAG + idx * FSTR; }
DI void publish(char* ws, u32 idx, u32 tag, int lane) {
  waitv<0>();
  if (lane == 0) __hip_atomic_store((u32*)(ws + flag_off(idx)), tag, __ATOMIC_RELAXED, __HIP_MEMORY_SCOPE_AGENT);
}
DI void pollf(char* ws, u32 idx, u32 tag) {
  const u32* f = (const u32*)(ws + flag_off(idx));
#pragma unroll 1
  for (int it = 0; it < MAXSPIN; ++it) {
    u32 v = __hip_atomic_load(f, __ATOMIC_RELAXED, __HIP_MEMORY_SCOPE_AGENT);
    if (__all(v == tag)) break;
    __builtin_amdgcn_s_sleep(1);
  }
  CFENCE();
}

DI float red_max_g(float x) {
  u32 u = __float_as_uint(x);
  auto r = __builtin_amdgcn_permlane16_swap(u, u, false, false);
  x = fmaxf(__uint_as_float(r[0]), __uint_as_float(r[1]));
  u = __float_as_uint(x);
  auto r2 = __builtin_amdgcn_permlane32_swap(u, u, false, false);
  return fmaxf(__uint_as_float(r2[0]), __uint_as_float(r2[1]));
}
DI float red_sum_g(float x) {
  u32 u = __float_as_uint(x);
  auto r = __builtin_amdgcn_permlane16_swap(u, u, false, false);
  x = __uint_as_float(r[0]) + __uint_as_float(r[1]);
  u = __float_as_uint(x);
  auto r2 = __builtin_amdgcn_permlane32_swap(u, u, false, false);
  return __uint_as_float(r2[0]) + __uint_as_float(r2[1]);
}
template <int CTRL> DI float dpp_add(float x) {
  return x + __uint_as_float(__builtin_amdgcn_update_dpp(0, __float_as_uint(x), CTRL, 0xf, 0xf, false));
}
DI float red_sum_all(float x) {
  x = dpp_add<0x128>(x); x = dpp_add<0x124>(x); x = dpp_add<0x122>(x); x = dpp_add<0x121>(x);
  return red_sum_g(x);
}

#define NPRE 3
#define LDECL
#define LST(k)
#define LST2(k)
#define LFLUSH(ws)
#define P2SLEEP 2
#define DBGCNT(x)
#define LD1 ld16
#define LD2 ld16
#define BPRIO2 0
#define APRIO2 2
#define APRIO 0
#define BPRIO 2
DI float amax3(float a, float b, float c) { float r; asm("v_max3_f32 %0, %1, %2, %3" : "=v"(r) : "v"(a), "v"(b), "v"(c)); return r; }
DI float anmin3(float a, float b, float c) { float r; asm("v_max3_f32 %0, -%1, -%2, -%3" : "=v"(r) : "v"(a), "v"(b), "v"(c)); return r; }
DI float amax2(float a, float b) { float r; asm("v_max_f32 %0, %1, %2" : "=v"(r) : "v"(a), "v"(b)); return r; }
template <bool MAX> DI void red2_g(float& a, float& b) {
  auto r = __builtin_amdgcn_permlane16_swap(__float_as_uint(a), __float_as_uint(b), false, false);
  float t = MAX ? amax2(__uint_as_float(r[0]), __uint_as_float(r[1])) : __uint_as_float(r[0]) + __uint_as_float(r[1]);
  auto r2 = __builtin_amdgcn_permlane32_swap(__float_as_uint(t), __float_as_uint(t), false, false);
  t = MAX ? amax2(__uint_as_float(r2[0]), __uint_as_float(r2[1])) : __uint_as_float(r2[0]) + __uint_as_float(r2[1]);
  auto r3 = __builtin_amdgcn_permlane16_swap(__float_as_uint(t), __float_as_uint(t), false, false);
  a = __uint_as_float(r3[0]); b = __uint_as_float(r3[1]);
}
DI void dual_softmax(const f32x4 (&pacc)[2], u32x4& pos, u32x4& neg) {
  constexpr float L2E = 1.4426950408889634f;
  float p[7];
  {
    u32 a = pk2(pacc[0][0], pacc[0][1]), b = pk2(pacc[0][2], pacc[0][3]), c = pk2(pacc[1][0], pacc[1][1]), d = pk2(pacc[1][2], pacc[1][2]);
    p[0] = bflo(a); p[1] = bfhi(a); p[2] = bflo(b); p[3] = bfhi(b); p[4] = bflo(c); p[5] = bfhi(c); p[6] = bflo(d);
  }
  float mx = amax3(amax3(p[0], p[1], p[2]), amax3(p[3], p[4], p[5]), p[6]);
  float nm = amax2(amax2(anmin3(p[0], p[1], p[2]), anmin3(p[3], p[4], p[5])), -p[6]);
  red2_g<true>(mx, nm);
  const f32x2 cc = {L2E, -L2E}, off = {-mx * L2E, -nm * L2E};
  f32x2 e[7];
#pragma unroll
  for (int i = 0; i < 7; ++i) {
    const f32x2 a = __builtin_elementwise_fma((f32x2){p[i], p[i]}, cc, off);
    e[i] = (f32x2){__builtin_amdgcn_exp2f(a.x), __builtin_amdgcn_exp2f(a.y)};
  }
  const f32x2 sm = ((e[0] + e[1]) + (e[2] + e[3])) + ((e[4] + e[5]) + e[6]);
  float sp = sm.x, sn = sm.y;
  red2_g<false>(sp, sn);
  const f32x2 rr = {__builtin_amdgcn_rcpf(sp), __builtin_amdgcn_rcpf(sn)};
#pragma unroll
  for (int i = 0; i < 7; ++i) e[i] *= rr;
#pragma unroll
  for (int i = 0; i < 3; ++i) { pos[i] = pk2(e[2 * i].x, e[2 * i + 1].x); neg[i] = pk2(e[2 * i].y, e[2 * i + 1].y); }
  pos[3] = pk2(e[6].x, 0.f); neg[3] = pk2(e[6].y, 0.f);
}
DI float acc_sel(u32 v, u32 sel, float a) { return __builtin_amdgcn_fdot2_f32_bf16(__builtin_bit_cast(bf16x2, v), __builtin_bit_cast(bf16x2, sel), a, false); }
DI bool tag_ok(u32 v3, u32 t16) { return __all((v3 >> 16) == t16); }

struct Params { const u16* in[14]; char* ws; u16* out; };

constexpr int A_X = 0, A_W = 32768, A_WPHI = 73728, A_GW = 75776, A_VT = 16384, A_Q6 = 24576, A_KVB = 76800, A_RED = 65536;
constexpr int LDS_BYTES = 81920;
constexpr int WROW = 3 * D * 2;
constexpr int WCH = 64 * WROW;

DI void roleA(const Params& p, int x8, int j, char* lds) {
  const int tid = threadIdx.x, lane = tid & 63, w = __builtin_amdgcn_readfirstlane(tid >> 6);
  const int g = lane >> 4, l15 = lane & 15, q4 = l15 >> 2, pp = lane & 3;
  const int h = 4 * (x8 & 3) + (j & 3), tile = 8 * (x8 >> 2) + (j >> 2);
  const int rot = (2 * (h & 3) + (tile & 7)) % 7;
  char* ws = p.ws;
  LDECL
  STAMP_DECL
  STAMP(0);
  __builtin_amdgcn_s_setprio(3);
  const u32x4 rX = mkrs4(p.in[0], S * D * 2), rW = mkrs4(p.in[1], D * 3 * D * 2);
  const rsrc_t rWS = mkrs(ws, WS_END);
  const u32x4 rWS4 = mkrs4(ws, WS_END);
  const u32 lb = ldsaddr(lds);
  const u32 gen = sload(ws + WS_GEN);
  const int tok0 = tile * 128 + w * 16;
  u32 xvo[2];
#pragma unroll
  for (int i = 0; i < 2; ++i) { int row = 8 * i + (lane >> 3); xvo[i] = (tok0 + row) * (D * 2) + (((lane & 7) ^ ((row >> 1) & 7)) * 16); }
  u32 wvo;
  { int row = 16 * (w & 3) + (lane >> 2); int cs = (lane & 3) ^ (2 * ((row >> 3) & 1)); wvo = row * WROW + h * (DH * 2) + (cs < 3 ? cs * 16 : 40); }
  auto cidx = [&](int i) { int c = rot + i; return c >= 7 ? c - 7 : c; };
  auto issueX = [&](int i) {
    const u32 so = cidx(i) * 128, dst = lb + A_X + (i & 1) * 16384 + w * 2048;
    adma16(rX, dst, xvo[0], so); adma16(rX, dst + 1024, xvo[1], so);
  };
  auto issueW = [&](int v) {
    const u32 dst = lb + A_W + (v % 5) * 8192 + w * 1024;
    if (v < 7) adma16(rW, dst, wvo, cidx(v) * WCH + (1 + (w >> 2)) * (D * 2));
    else adma16(rW, dst, wvo, cidx(2 * (v - 7) + (w >> 2)) * WCH);
  };
  if (w < 2) adma16(mkrs4(p.in[2], H * DH * DH * 2), lb + A_WPHI + w * 1024, lane * 16, h * (DH * DH * 2) + w * 1024);
  else if (w == 2) { int c = rot + (lane >> 3); c = c >= 7 ? c - 7 : c; adma16(mkrs4(p.in[13], D * 2), lb + A_GW, lane < 56 ? c * 128 + (lane & 7) * 16 : 4096, 0); }
  issueW(0); issueX(0);
  __builtin_amdgcn_s_setprio(2);
  issueW(1); issueX(1);
  __builtin_amdgcn_s_setprio(1);
  issueW(2);
  __builtin_amdgcn_s_setprio(APRIO);
  __builtin_amdgcn_sched_barrier(0);
  STAMP(1);
  f32x4 ak[2] = {{0, 0, 0, 0}, {0, 0, 0, 0}}, av[2] = {{0, 0, 0, 0}, {0, 0, 0, 0}};
  u32 xoff[2];
#pragma unroll
  for (int ks = 0; ks < 2; ++ks) xoff[ks] = w * 2048 + l15 * 128 + (((4 * ks + g) ^ ((l15 >> 1) & 7)) * 16);
  u32 woff[2];
#pragma unroll
  for (int ct = 0; ct < 2; ++ct) woff[ct] = (8 * g + q4) * 64 + ((ct ^ (g & 1)) * 32) + pp * 8;
  u32x4 xf[14];
  const bool isg = w == (h >> 1) && ((h ^ tile) & 1) == 0;
  f32x4 ga = {0, 0, 0, 0};
  u32x4 wf0[2][2], wf1[2][2];
  auto readX = [&](int i) {
    const char* xs = lds + A_X + (i & 1) * 16384;
    xf[2 * i] = *(const u32x4*)(xs + xoff[0]); xf[2 * i + 1] = *(const u32x4*)(xs + xoff[1]);
  };
  auto readW = [&](int i, int ks, u32x4 (&wf)[2][2]) {
    const char* wsb = lds + A_W + (i % 5) * 8192;
#pragma unroll
    for (int r = 0; r < 2; ++r)
#pragma unroll
      for (int ct = 0; ct < 2; ++ct) {
        u32x2 a = trread(wsb, woff[ct] + r * 4096 + ks * 2048);
        u32x2 b = trread(wsb, woff[ct] + r * 4096 + ks * 2048 + 256);
        wf[r][ct] = (u32x4){a[0], a[1], b[0], b[1]};
      }
  };
  auto mm = [&](const u32x4& x, const u32x4 (&wf)[2][2]) {
#pragma unroll
    for (int ct = 0; ct < 2; ++ct) { ak[ct] = mfma16(wf[0][ct], x, ak[ct]); av[ct] = mfma16(x, wf[1][ct], av[ct]); }
  };
  waitv<3>();
  BAR();
  CFENCE();
  readX(0); readW(0, 0, wf0);
  __builtin_amdgcn_sched_barrier(0);
  readW(0, 1, wf1);
  __builtin_amdgcn_sched_barrier(0);
  WAIT_L(8);
  issueX(2); issueW(3);
#pragma unroll
  for (int i = 0; i < 7; ++i) {
    __builtin_amdgcn_sched_barrier(0);
    mm(xf[2 * i], wf0);
    __builtin_amdgcn_sched_barrier(0);
    if (i < 6) {
      if (i == 0 || i == 3 || i == 4) waitv<4>(); else if (i == 5) { if (w < 4) waitv<3>(); else waitv<2>(); } else waitv<5>();
      LST(2 * i);
      readX(i + 1); readW(i + 1, 0, wf0);
    }
    __builtin_amdgcn_sched_barrier(0);
    mm(xf[2 * i + 1], wf1);
    if (isg) {
      ga = mfma16(*(const u32x4*)(lds + A_GW + i * 128 + g * 16), xf[2 * i], ga);
      ga = mfma16(*(const u32x4*)(lds + A_GW + i * 128 + 64 + g * 16), xf[2 * i + 1], ga);
    }
    __builtin_amdgcn_sched_barrier(0);
    if (i == 1 && w == 7) {
      u32x4 wph[2];
#pragma unroll
      for (int ft = 0; ft < 2; ++ft) {
        int f = 7 * q4 + 4 * ft + pp; f = f > 27 ? 27 : f;
        const char* wp = lds + A_WPHI + f * 2;
        const int d0 = 4 * g, d1 = g < 2 ? 16 + 4 * g : 24;
        u32 e[8];
#pragma unroll
        for (int jj = 0; jj < 4; ++jj) { e[jj] = *(const u16*)(wp + (d0 + jj) * 56); e[4 + jj] = *(const u16*)(wp + (d1 + jj) * 56); }
        wph[ft] = (u32x4){e[0] | (e[1] << 16), e[2] | (e[3] << 16), g == 2 ? 0u : (e[4] | (e[5] << 16)), g == 2 ? 0u : (e[6] | (e[7] << 16))};
      }
      CFENCE();
      *(u32x4*)(lds + A_WPHI + lane * 16) = wph[0]; *(u32x4*)(lds + A_WPHI + 1024 + lane * 16) = wph[1];
      WAIT_L(0);
    }
    if (i < 6) {
      readW(i + 1, 1, wf1);
      __builtin_amdgcn_sched_barrier(0);
      WAIT_L(8);
      if (i + 3 < 7) issueX(i + 3);
      if (i == 0) issueW(4);
      if (i < 5) {
        if (i < 3) waitv<6>();
        LST(2 * i + 1);
        BAR();
        CFENCE();
        LST2(i);
        issueW(i + 5);
        if (i == 4 && w < 4) adma16(rW, lb + A_Q6 + w * 1024, wvo, cidx(6) * WCH);
      }
    }
  }
  __builtin_amdgcn_s_setprio(APRIO2);
  STAMP(2);
  const u32 tag = gen + 1u, t16 = tag & 0xffffu;
  char* phk = lds + A_X; char* vt = lds + A_VT;
  {
    const int tl = w * 16 + l15;
    u32x4 kb = {pk2(ak[0][0], ak[0][1]), pk2(ak[0][2], ak[0][3]), pk2(ak[1][0], ak[1][1]), pk2(ak[1][2], ak[1][3])};
    f32x4 z4 = {0, 0, 0, 0}; f32x4 pk[2];
    pk[0] = mfma16(*(const u32x4*)(lds + A_WPHI + lane * 16), kb, z4); pk[1] = mfma16(*(const u32x4*)(lds + A_WPHI + 1024 + lane * 16), kb, z4);
    u32x4 pos, neg; dual_softmax(pk, pos, neg);
    const int sw = ((tl >> 1) & 1) | (((tl >> 3) & 1) << 1);
    char* rowp = phk + tl * 128 + g * 8;
    *(u32x2*)(rowp + ((0 ^ sw) * 32)) = (u32x2){pos[0], pos[1]};
    *(u32x2*)(rowp + ((1 ^ sw) * 32)) = (u32x2){pos[2], pos[3]};
    *(u32x2*)(rowp + ((2 ^ sw) * 32)) = (u32x2){neg[0], neg[1]};
    *(u32x2*)(rowp + ((3 ^ sw) * 32)) = (u32x2){neg[2], neg[3]};
  }
  STAMP(3);
  {
#pragma unroll
    for (int ct = 0; ct < 2; ++ct) {
      int d = ct == 0 ? l15 : (l15 < 8 ? 16 + l15 : 12 + l15);
      int G = (d * 37) >> 8, e = d - 7 * G;
      int rho = e < 4 ? 4 * G + e : 12 + 4 * G + e;
      f32x4 v = av[ct];
      if (ct == 1 && (l15 >> 2) == 2) { rho = pp == 0 ? 27 : (pp == 1 ? 19 : (pp == 2 ? 23 : 31)); if (pp == 0) v = (f32x4){1.f, 1.f, 1.f, 1.f}; }
      *(u32x2*)(vt + rho * 256 + (((2 * w + (g >> 1)) ^ (rho & 15)) * 16) + (g & 1) * 8) = (u32x2){pk2(v[0], v[1]), pk2(v[2], v[3])};
    }
  }
  waitv<0>();
  bar_l();
  STAMP(4);
  if (isg) {
    const float gate = rbf(__builtin_amdgcn_rcpf(1.f + __expf(-rbf(ga[0]))));
    if (lane < 16) st4c((__float_as_uint(gate) >> 16) | (t16 << 16), rWS, (tok0 + lane) * 4, WS_GATE);
  }
  if (w < 4) {
    const int sg = w >> 1, dt = w & 1;
    f32x4 t0 = {0, 0, 0, 0}, t1 = t0;
#pragma unroll
    for (int ks = 0; ks < 4; ++ks) {
      u32x4 af[2];
#pragma unroll
      for (int ft = 0; ft < 2; ++ft) {
        u32x2 a[2];
#pragma unroll
        for (int t = 0; t < 2; ++t) {
          int row = 32 * ks + 8 * g + 4 * t + q4;
          int sw = ((row >> 1) & 1) | (((row >> 3) & 1) << 1);
          a[t] = trread(phk, row * 128 + (((2 * sg + ft) ^ sw) * 32) + pp * 8);
        }
        af[ft] = (u32x4){a[0][0], a[0][1], a[1][0], a[1][1]};
      }
      u32x4 bf = *(const u32x4*)(vt + (16 * dt + l15) * 256 + (((4 * ks + g) ^ l15) * 16));
      t0 = mfma16(af[0], bf, t0); t1 = mfma16(af[1], bf, t1);
    }
    u32x4 un = {pk2(t0[0], t0[1]), pk2(t0[2], t0[3]), pk2(t1[0], t1[1]), (pk2(t1[2], t1[2]) & 0xffffu) | (t16 << 16)};
    st16c(un, rWS, (((h * 16 + tile) * 4 + w) * 64 + lane) * 16, WS_KVP);
  }
  STAMP(5);
  u32x4 pqp, pqn;
  f32x4 pq[2];
  u32x4 pv[16];
  const u32 pvo = ((h * 16 * 4 + (w & 3)) * 64 + lane) * 16;
  const u32 plo = WS_KVP + (((h * 16 + l15) * 4 + (w & 3)) * 64 + 63) * 16 + 12;
  u32 pl = 0;
  u32 done = 0;
  const u32 r0 = (tile & 3) * 4;
  auto stepPV = [&]() {
    u32 m = (u32)__ballot((pl >> 16) == t16) & 0xffffu;
    m = ((m >> r0) | (m << (16 - r0))) & 0xffffu;
    const u32 nw = m & ~done;
    done |= nw;
#pragma unroll
    for (int t = 0; t < 16; ++t)
      if (nw & (1u << t)) ald16io(pv[t], rWS4, pvo, WS_KVP + ((t + r0) & 15) * 4096);
    if (done != 0xffffu) pl = ld4c(rWS, plo, 0);
  };
  {
    f32x4 aq[2] = {{0, 0, 0, 0}, {0, 0, 0, 0}};
#pragma unroll
    for (int i = 0; i < 7; ++i) {
      const char* qb = lds + (i == 6 ? A_Q6 : A_W + ((7 + (i >> 1)) % 5) * 8192 + (i & 1) * 4096);
      if (i == 4 && w >= 4) pl = ld4c(rWS, plo, 0);
#pragma unroll
      for (int ks = 0; ks < 2; ++ks)
#pragma unroll
        for (int ct = 0; ct < 2; ++ct) {
          u32x2 a = trread(qb, woff[ct] + ks * 2048);
          u32x2 b = trread(qb, woff[ct] + ks * 2048 + 256);
          aq[ct] = mfma16((u32x4){a[0], a[1], b[0], b[1]}, xf[2 * i + ks], aq[ct]);
        }
    }
    const u32x4 wq0 = *(const u32x4*)(lds + A_WPHI + lane * 16), wq1 = *(const u32x4*)(lds + A_WPHI + 1024 + lane * 16);
    u32x4 qb = {pk2(aq[0][0], aq[0][1]), pk2(aq[0][2], aq[0][3]), pk2(aq[1][0], aq[1][1]), pk2(aq[1][2], aq[1][3])};
    f32x4 z4 = {0, 0, 0, 0};
    pq[0] = mfma16(wq0, qb, z4); pq[1] = mfma16(wq1, qb, z4);
    __builtin_amdgcn_sched_barrier(0);
  }
  const bool fin = j < 28;
  const int tt = 4 * x8 + j / 7, cg = j % 7, c0 = cg * 64;
  const int rt = w >> 1, kh = w & 1;
  const int ftok = tt * 64 + rt * 16 + l15;
  if (w < 4) {
    dual_softmax(pq, pqp, pqn);
    STAMP(6);
  } else {
    undef8(pv); undef8(pv + 8); stepPV();
    __builtin_amdgcn_sched_barrier(0);
    dual_softmax(pq, pqp, pqn);
    __builtin_amdgcn_sched_barrier(0);
    STAMP(6);
#pragma unroll 1
    for (int it = 0; it < MAXSPIN && done != 0xffffu; ++it) stepPV();
    wait8(pv); wait8(pv + 8);
    u32 pend = 15;
#pragma unroll 1
    for (int it = 0; it < MAXSPIN; ++it) {
#pragma unroll
      for (int gq = 0; gq < 4; ++gq)
        if (pend & (1u << gq)) {
          const u32 m = (pv[4 * gq][3] ^ (t16 << 16)) | (pv[4 * gq + 1][3] ^ (t16 << 16)) | (pv[4 * gq + 2][3] ^ (t16 << 16)) | (pv[4 * gq + 3][3] ^ (t16 << 16));
          if (__all(m < 0x10000u)) pend &= ~(1u << gq);
        }
      if (!pend) break;
      __builtin_amdgcn_s_sleep(2);
#pragma unroll
      for (int gq = 0; gq < 4; ++gq)
        if (pend & (1u << gq)) {
#pragma unroll
          for (int t = 0; t < 4; ++t) ald16cio(pv[4 * gq + t], rWS4, pvo, WS_KVP + ((4 * gq + t + r0) & 15) * 4096);
        }
      wait8(pv); wait8(pv + 8);
    }
    float s[7] = {0, 0, 0, 0, 0, 0, 0};
    u32 selo = 0x00003f80u, sehi = 0x3f800000u;
    asm volatile("" : "+s"(selo), "+s"(sehi));
#pragma unroll
    for (int t = 0; t < 16; ++t) {
      s[0] = acc_sel(pv[t][0], selo, s[0]); s[1] = acc_sel(pv[t][0], sehi, s[1]); s[2] = acc_sel(pv[t][1], selo, s[2]); s[3] = acc_sel(pv[t][1], sehi, s[3]);
      s[4] = acc_sel(pv[t][2], selo, s[4]); s[5] = acc_sel(pv[t][2], sehi, s[5]); s[6] += bflo(pv[t][3]);
    }
    *(u32x4*)(lds + A_KVB + (w & 3) * 1024 + lane * 16) = (u32x4){pk2(s[0], s[1]), pk2(s[2], s[3]), pk2(s[4], s[5]), pk2(s[6], 0.f)};
  }
  bar_l();
  STAMP(7);
  {
    f32x4 o[2];
#pragma unroll
    for (int dt = 0; dt < 2; ++dt) {
      const u32x4 kp = *(const u32x4*)(lds + A_KVB + dt * 1024 + lane * 16), kn = *(const u32x4*)(lds + A_KVB + (2 + dt) * 1024 + lane * 16);
      o[dt] = mfma16(kp, pqp, (f32x4){0, 0, 0, 0});
      o[dt] = mfma16(kn, pqn, o[dt]);
    }
    u32 du = __float_as_uint(o[1][3]);
    auto r1 = __builtin_amdgcn_permlane32_swap(du, du, false, false);
    u32 d1 = r1[1];
    auto r2 = __builtin_amdgcn_permlane16_swap(d1, d1, false, false);
    float den = __uint_as_float(r2[0]);
    den = rbf(rbf(den) + 1e-6f);
    const float rd = __builtin_amdgcn_rcpf(den);
    const u32 n0 = pk2(o[0][0], o[0][1]), n1 = pk2(o[0][2], o[0][3]), n2 = pk2(o[1][0], o[1][1]), n3 = pk2(o[1][2], o[1][2]);
    u32x4 of = {pk2(bflo(n0) * rd, bfhi(n0) * rd), pk2(bflo(n1) * rd, bfhi(n1) * rd), pk2(bflo(n2) * rd, bfhi(n2) * rd), (pk2(bflo(n3) * rd, 0.f) & 0xffffu) | (t16 << 16)};
    const int rt16 = tile * 8 + w;
    st16c(of, rWS, ((rt16 * 16 + h) * 64 + lane) * 16, WS_OF);
  }
  STAMP(8);
  if (fin) {
    const int rt16 = tt * 4 + rt;
    {
      __builtin_amdgcn_sched_barrier(0);
      const u32x4 rWO = mkrs4(p.in[3], D * D * 2);
      const int jr = lane >> 3;
      const u32 vo = (jr < 7 ? jr : 6) * (D * 2) + cg * 128 + (((lane & 7) ^ (2 * (((jr >> 1) & 1) | ((w & 1) << 1)))) * 16);
#pragma unroll
      for (int m = 0; m < 8; ++m) adma16(rWO, lb + (w + 8 * m) * 1024, vo, (w + 8 * m) * (7 * D * 2));
      waitv<0>();
      BAR();
      CFENCE();
    }
    u32 wa[4];
    { const int s = 2 * (((q4 >> 1) & 1) | ((g & 1) << 1));
#pragma unroll
      for (int ct = 0; ct < 4; ++ct) wa[ct] = kh * 32768 + (8 * g + q4) * 128 + (((2 * ct) ^ s) * 16) + pp * 8; }
    u32x4 wf[NPRE][4];
    auto readWO = [&](int hh, u32x4 (&f)[4]) {
#pragma unroll
      for (int ct = 0; ct < 4; ++ct) {
        u32x2 a = trread(lds, wa[ct] + hh * 4096);
        u32x2 b = trread(lds, wa[ct] + hh * 4096 + 512);
        f[ct] = (u32x4){a[0], a[1], b[0], b[1]};
      }
    };
#pragma unroll
    for (int hh = 0; hh < NPRE; ++hh) readWO(hh, wf[hh]);
    __builtin_amdgcn_sched_barrier(0);
    u32x4 of[8];
    u32 yv[2][4], gv;
    const u32 ovo = ((rt16 * 16 + 8 * kh) * 64 + lane) * 16;
    const u32 yvo = (c0 + 32 * kh + 4 * g) * (S * 4) + ftok * 4;
    const u32 tw = t16 << 16;
    {
      const u32 po = lane < 8 ? WS_OF + ((rt16 * 16 + 8 * kh + lane) * 64 + 63) * 16 + 12 : WS_GATE + (rt16 * 16 + 15) * 4;
      u32 dn = 0;
      undef8(of);
#pragma unroll 1
      for (int it = 0; it < MAXSPIN; ++it) {
        const u32 v = ld4c(rWS, po, 0);
        const u32 m = (u32)__ballot((v >> 16) == t16) & 0x1ffu, nw = m & ~dn;
        dn |= nw;
#pragma unroll
        for (int t = 0; t < 8; ++t)
          if (nw & (1u << t)) ald16io(of[t], rWS4, ovo, WS_OF + t * 1024);
        if (dn == 0x1ffu) break;
        __builtin_amdgcn_s_sleep(P2SLEEP);
      }
    }
    DBGCNT(st_[12] = (u32)__builtin_amdgcn_s_memrealtime();)
    gv = ld4c(rWS, ftok * 4, WS_GATE);
    auto loadY = [&]() {
#pragma unroll
      for (int m = 0; m < 2; ++m)
#pragma unroll
        for (int i = 0; i < 4; ++i) yv[m][i] = ld4c(rWS, yvo, WS_YT + (16 * m + i) * (S * 4));
    };
    loadY();
    asm volatile("s_waitcnt vmcnt(9)" : "+v"(of[0]), "+v"(of[1]), "+v"(of[2]), "+v"(of[3]), "+v"(of[4]), "+v"(of[5]), "+v"(of[6]), "+v"(of[7])::"memory");
#pragma unroll 1
    for (int it = 0; it < MAXSPIN; ++it) {
      u32 m = gv ^ tw;
#pragma unroll
      for (int t = 0; t < 8; ++t) m |= of[t][3] ^ tw;
      if (__all(m < 0x10000u)) break;
      __builtin_amdgcn_s_sleep(2);
#pragma unroll
      for (int t = 0; t < 8; ++t) ald16cio(of[t], rWS4, ovo, WS_OF + t * 1024);
      gv = ld4c(rWS, ftok * 4, WS_GATE);
      wait8(of);
    }
    STAMP(9);
    f32x4 acc[4];
#pragma unroll
    for (int ct = 0; ct < 4; ++ct) acc[ct] = (f32x4){0, 0, 0, 0};
#pragma unroll
    for (int hh = 0; hh < 8; ++hh) {
      u32x4 ob = of[hh]; ob[3] &= 0xffffu;
#pragma unroll
      for (int ct = 0; ct < 4; ++ct) acc[ct] = mfma16(wf[hh % NPRE][ct], ob, acc[ct]);
      if (hh + NPRE < 8) readWO(hh + NPRE, wf[hh % NPRE]);
      __builtin_amdgcn_sched_barrier(0);
    }
    STAMP(10);
    const f32x4 c0_ = acc[0], c1_ = acc[1], c2_ = acc[2], c3_ = acc[3];
    {
      f32x4* rw = (f32x4*)(lds + A_RED + w * 2048) + lane;
      rw[0] = kh ? c0_ : c2_; rw[64] = kh ? c1_ : c3_;
    }
    const f32x4 keep0 = kh ? c2_ : c0_, keep1 = kh ? c3_ : c1_;
    bar_l();
#pragma unroll 1
    for (int it = 0; it < MAXSPIN; ++it) {
      const u32 m = (yv[0][0] ^ tw) | (yv[0][1] ^ tw) | (yv[0][2] ^ tw) | (yv[0][3] ^ tw) | (yv[1][0] ^ tw) | (yv[1][1] ^ tw) | (yv[1][2] ^ tw) | (yv[1][3] ^ tw);
      if (__all(m < 0x10000u)) break;
      __builtin_amdgcn_s_sleep(4);
      loadY();
    }
    DBGCNT(st_[3] = (u32)__builtin_amdgcn_s_memrealtime();)
    const f32x4* rr = (const f32x4*)(lds + A_RED + (w ^ 1) * 2048) + lane;
    const float gg = bflo(gv), og = rbf(1.f - gg);
    u32 res[2][2];
#pragma unroll
    for (int m = 0; m < 2; ++m) {
      f32x4 a = rr[m * 64];
      a += m ? keep1 : keep0;
      float r[4];
#pragma unroll
      for (int i = 0; i < 4; ++i) {
        float av_ = rbf(a[i]);
        r[i] = rbf(smul(gg, av_)) + rbf(smul(og, bflo(yv[m][i])));
      }
      res[m][0] = pk2(r[0], r[1]); res[m][1] = pk2(r[2], r[3]);
    }
    u32x4 ov;
    {
      auto s0 = __builtin_amdgcn_permlane32_swap(res[0][0], res[1][0], false, false);
      auto s1 = __builtin_amdgcn_permlane32_swap(res[0][1], res[1][1], false, false);
      auto t0 = __builtin_amdgcn_permlane16_swap(s0[0], s0[1], false, false);
      auto t1 = __builtin_amdgcn_permlane16_swap(s1[0], s1[1], false, false);
      ov = (u32x4){t0[0], t1[0], t0[1], t1[1]};
    }
    *(u32x4*)(p.out + (size_t)ftok * D + c0 + 32 * kh + 8 * g) = ov;
    STAMP(11);
  }
  else if (x8 == 0 && j == 31) {
    if (tid < 256) {
      const u32 off = tid < 224 ? WS_YT + 2 * tid * (S * 4) : WS_OF + ((tid - 224) & 15) * 1024 + 12;
#pragma unroll 1
      for (int it = 0; it < MAXSPIN; ++it) {
        const u32 v = ld4c(rWS, off, 0);
        if (__all((v >> 16) == t16)) break;
        __builtin_amdgcn_s_sleep(16);
      }
    }
    CFENCE();
    BAR();
    if (tid == 0) __hip_atomic_store((u32*)(ws + WS_GEN), tag, __ATOMIC_RELAXED, __HIP_MEMORY_SCOPE_AGENT);
  }
  LFLUSH(ws);
  STAMP_FLUSH(ws);
}

DI f32x2 cmul(f32x2 a, f32x2 b) {
  float t0, t1, re, im;
  asm("v_mul_f32 %0, %1, %2" : "=v"(t0) : "v"(a.y), "v"(b.y));
  asm("v_mul_f32 %0, %1, %2" : "=v"(t1) : "v"(a.y), "v"(b.x));
  asm("v_fma_f32 %0, %1, %2, -%3" : "=v"(re) : "v"(a.x), "v"(b.x), "v"(t0));
  asm("v_fma_f32 %0, %1, %2, %3" : "=v"(im) : "v"(a.x), "v"(b.y), "v"(t1));
  return (f32x2){re, im};
}
DI f32x2 cexp_pow(float mr, float pr, float a) {
  float m = __builtin_amdgcn_exp2f(mr * a);
  float t = __builtin_amdgcn_fractf(pr * a);
  return (f32x2){m * __builtin_amdgcn_cosf(t), m * __builtin_amdgcn_sinf(t)};
}
constexpr int KG_ROW = 272, KG_SLAB = 16 * KG_ROW;
constexpr int B_UC = 34816, UCH = 16656, B_KR = 68128, B_KTAB = 77344, B_DUM = 80416, B_TIMG = B_KR, B_RED = 0;
constexpr int UC0 = 8, UC1 = 4176, UC2 = 8336, UC3 = 12496;

DI void roleB(const Params& p, int x8, int jb, char* lds) {
  const int tid = threadIdx.x, lane = tid & 63, w = __builtin_amdgcn_readfirstlane(tid >> 6);
  const int g = lane >> 4, l15 = lane & 15;
  char* ws = p.ws;
  STAMP_DECL
  STAMP(0);
  __builtin_amdgcn_s_setprio(3);
  const rsrc_t rWS = mkrs(ws, WS_END);
  const int tt = 4 * x8 + jb / 7, cg = jb % 7, ch0 = 2 * (x8 * 28 + jb);
  const u32 gen = sload(ws + WS_GEN);
  if (w == 7) {
    const u32x4 rc = mkrs4((const void*)(__builtin_amdgcn_s_getpc() & ~(unsigned long long)127), CODE_BYTES);
#pragma unroll
    for (int i = 0; i < (CODE_BYTES + 8191) / 8192; ++i) atouch4(rc, ldsaddr(lds) + B_DUM, lane * 128, i * 8192);
  } else if (w >= 5) {
    atouch4(mkrs4(p.in[3], D * D * 2), ldsaddr(lds) + B_DUM, lane * 128, (jb * 2 + (w - 5)) * 7168);
  }
  const u32x4 xv = *(const u32x4*)(p.in[0] + (size_t)(tt * 64 + (tid >> 3)) * D + cg * 64 + (tid & 7) * 8);
  const int d = ch0 + (w >> 2);
  const int mo = d * NM + (w & 3) * 64 + lane;
  const u32 are = p.in[5][mo], aim = p.in[6][mo], bre = p.in[7][mo], bim = p.in[8][mo], cre = p.in[9][mo], cim = p.in[10][mo];
  const u32 ldt = p.in[11][d], dskip = p.in[12][d];
  __builtin_amdgcn_sched_barrier(0);
  const u32 tag = gen + 1u, tw = (tag & 0xffffu) << 16;
  {
    char* img = lds + B_TIMG;
    u32* dst = (u32*)(img + (tid >> 3) * 132 + (tid & 7) * 16);
    dst[0] = xv[0]; dst[1] = xv[1]; dst[2] = xv[2]; dst[3] = xv[3];
    bar_l();
    const int chl = tid >> 3, oc = tid & 7;
    u32 o[8];
#pragma unroll
    for (int jj = 0; jj < 8; ++jj) o[jj] = *(const u16*)(img + (4 * oc + (jj & 3) + 32 * (jj >> 2)) * 132 + chl * 2) | tw;
    st16c((u32x4){o[0], o[1], o[2], o[3]}, rWS, (cg * 64 + chl) * (S * 4) + tt * 256 + oc * 16, WS_XT);
    st16c((u32x4){o[4], o[5], o[6], o[7]}, rWS, (cg * 64 + chl) * (S * 4) + tt * 256 + oc * 16 + 128, WS_XT);
  }
  __builtin_amdgcn_s_setprio(BPRIO);
  STAMP(1);
  char* slab = lds + w * KG_SLAB;
  const int cch = tid >> 8, cq = tid & 255;
  u32x4 ua, ub, na, nb;
  const u32 uvo = (ch0 + cch) * (S * 4) + cq * 16;
  auto loadU = [&]() {
    ua = ld16c(rWS, uvo, WS_XT); ub = ld16c(rWS, uvo + 4096, WS_XT);
    na = ua; nb = ub;
    if (lane == 63) { na = ld16c(rWS, uvo + 16, WS_XT); nb = ld16c(rWS, cq < 255 ? uvo + 4096 + 16 : uvo, WS_XT); }
  };
  f32x4 acc[3];
  {
    const float dt = rbf(__expf(bflo(ldt)));
    const float ar = dt * bflo(are), ai = dt * bflo(aim), br = dt * bflo(bre), bi = dt * bflo(bim), cr = bflo(cre), ci = bflo(cim);
    const float mr = ar * 1.4426950408889634f, pr = ai * 0.15915494309189535f;
    f32x2 W = {cr * br - ci * bi, -(cr * bi + ci * br)};
    f32x2 z1c = cexp_pow(mr, -pr, 1.f);
    f32x2 z8 = cexp_pow(mr, pr, 8.f);
    f32x2 a128 = cexp_pow(mr, -pr, 128.f);
    asm volatile("s_mov_b32 m0, %0" ::"s"(ldsaddr(slab)) : "memory");
#define WROWT(r, v) do { const f32x2 v_ = (v); asm volatile("ds_write_addtid_b32 %0 offset:%1" ::"v"(pk2(v_.x, v_.y)), "i"((r) * KG_ROW) : "memory"); } while (0)
    const char* rd = slab + l15 * KG_ROW + g * 16;
    u32x4 yf[4];
    {
      f32x2 y[16];
      y[1] = z8;
      f32x2 y1c = z8; asm volatile("" : "+v"(y1c));
      y[2] = cmul(y[1], y1c); y[3] = cmul(y[2], y[1]); y[4] = cmul(y[3], y[1]);
      y[5] = cmul(y[4], y[1]); y[6] = cmul(y[4], y[2]); y[7] = cmul(y[4], y[3]); y[8] = cmul(y[5], y[3]);
#pragma unroll
      for (int c = 9; c < 16; ++c) y[c] = cmul(y[8], y[c - 8]);
      WROWT(0, ((f32x2){1.f, 0.f}));
#pragma unroll
      for (int c = 1; c < 16; ++c) WROWT(c, y[c]);
    }
#pragma unroll
    for (int ks = 0; ks < 4; ++ks) yf[ks] = *(const u32x4*)(rd + ks * 64);
    CFENCE();
    f32x2 R0[8];
    {
      f32x2 zc = z1c; asm volatile("" : "+v"(zc));
      f32x2 z2 = cmul(z1c, zc);
      f32x2 z2c = z2; asm volatile("" : "+v"(z2c));
      f32x2 z4 = cmul(z2, z2c);
      R0[0] = W; R0[1] = cmul(W, z1c); R0[2] = cmul(W, z2); R0[3] = cmul(R0[1], z2);
#pragma unroll
      for (int b = 0; b < 4; ++b) R0[4 + b] = cmul(R0[b], z4);
    }
    f32x2 A[17];
    {
      A[1] = a128;
      f32x2 a1c = a128; asm volatile("" : "+v"(a1c));
      A[2] = cmul(A[1], a1c); A[3] = cmul(A[2], A[1]); A[4] = cmul(A[3], A[1]);
      A[5] = cmul(A[4], A[1]); A[6] = cmul(A[4], A[2]); A[7] = cmul(A[4], A[3]); A[8] = cmul(A[5], A[3]);
#pragma unroll
      for (int m = 9; m < 16; ++m) A[m] = cmul(A[8], A[m - 8]);
      A[16] = cmul(A[9], A[7]);
    }
    {
      f32x2 t[8];
#pragma unroll
      for (int b = 0; b < 8; ++b) t[b] = cmul(R0[b], A[1]);
#pragma unroll
      for (int b = 0; b < 8; ++b) { WROWT(b, R0[b]); WROWT(8 + b, t[b]); }
    }
    acc[0] = (f32x4){0, 0, 0, 0};
#pragma unroll
    for (int ks = 0; ks < 4; ++ks) { u32x4 xf = *(const u32x4*)(rd + ks * 64); acc[0] = mfma16(xf, yf[ks], acc[0]); }
    CFENCE();
    STAMP(7);
    {
      f32x2 t[16];
#pragma unroll
      for (int hh = 0; hh < 2; ++hh)
#pragma unroll
        for (int b = 0; b < 4; ++b) t[b + 4 * hh] = cmul(R0[2 * b], A[2 + hh]);
#pragma unroll
      for (int hh = 0; hh < 4; ++hh)
#pragma unroll
        for (int b = 0; b < 2; ++b) t[8 + b + 2 * hh] = cmul(R0[4 * b], A[4 + hh]);
#pragma unroll
      for (int r = 0; r < 16; ++r) WROWT(r, t[r]);
    }
    acc[1] = (f32x4){0, 0, 0, 0};
#pragma unroll
    for (int ks = 0; ks < 4; ++ks) { u32x4 xf = *(const u32x4*)(rd + ks * 64); acc[1] = mfma16(xf, yf[ks], acc[1]); }
    CFENCE();
    STAMP(9);
    __builtin_amdgcn_s_setprio(3);
    loadU();
    __builtin_amdgcn_s_setprio(BPRIO);
    __builtin_amdgcn_sched_barrier(0);
    {
      f32x2 t[9];
#pragma unroll
      for (int hh = 0; hh < 9; ++hh) t[hh] = cmul(W, A[8 + hh]);
#pragma unroll
      for (int hh = 0; hh < 9; ++hh) WROWT(hh, t[hh]);
    }
    acc[2] = (f32x4){0, 0, 0, 0};
#pragma unroll
    for (int ks = 0; ks < 4; ++ks) { u32x4 xf = *(const u32x4*)(rd + ks * 64); acc[2] = mfma16(xf, yf[ks], acc[2]); }
    CFENCE();
    float* kt = (float*)slab;
    *(f32x4*)(kt + 8 * l15 + 4 * (g & 1) + 128 * (g >> 1)) = acc[0];
    {
      const int t0 = g < 2 ? 256 + 4 * l15 + 64 * g : 384 + 2 * l15 + 64 * (g - 2);
      const int t1 = g < 2 ? t0 + 2 : t0 + 32;
      *(f32x2*)(kt + t0) = (f32x2){acc[1][0], acc[1][1]};
      *(f32x2*)(kt + t1) = (f32x2){acc[1][2], acc[1][3]};
    }
#pragma unroll
    for (int i = 0; i < 4; ++i) kt[512 + l15 + 16 * (4 * g + i)] = acc[2][i];
  }
  STAMP(2);
  {
    __builtin_amdgcn_s_setprio(3);
#pragma unroll 1
    for (int it = 0; it < MAXSPIN; ++it) {
      const u32 m = (ua[0] ^ tw) | (ua[1] ^ tw) | (ua[2] ^ tw) | (ua[3] ^ tw) | (ub[0] ^ tw) | (ub[1] ^ tw) | (ub[2] ^ tw) | (ub[3] ^ tw) | (na[0] ^ tw) | (na[1] ^ tw) | (na[2] ^ tw) | (nb[0] ^ tw) | (nb[1] ^ tw) | (nb[2] ^ tw);
      if (__all(m < 0x10000u)) break;
      __builtin_amdgcn_s_sleep(2);
      loadU();
    }
    __builtin_amdgcn_s_setprio(BPRIO);
    STAMP(10);
    char* base = lds + B_UC + cch * UCH + (16 + cq * 4) * 2;
    u32 e00, sh00;
#pragma unroll
    for (int hf = 0; hf < 2; ++hf) {
      const u32x4 uu = hf ? ub : ua, nn = hf ? nb : na;
      const u32 e0 = __builtin_amdgcn_perm(uu[1], uu[0], 0x05040100u), e1 = __builtin_amdgcn_perm(uu[3], uu[2], 0x05040100u);
      u32 f0 = __builtin_amdgcn_perm(nn[1], nn[0], 0x05040100u), f1 = nn[2] & 0xffffu;
      if (hf == 1 && cq == 255) { f0 = 0; f1 = 0; }
      const u32 n0 = __builtin_amdgcn_update_dpp(f0, e0, 0x130, 0xf, 0xf, false), n1 = __builtin_amdgcn_update_dpp(f1, e1, 0x130, 0xf, 0xf, false);
      const u32 s0 = __builtin_amdgcn_alignbit(e1, e0, 16), s1 = __builtin_amdgcn_alignbit(n0, e1, 16), s2 = __builtin_amdgcn_alignbit(n1, n0, 16);
      char* bh = base + hf * 2048;
      *(u32x2*)(bh + UC0) = (u32x2){e0, e1};
      *(u32x2*)(bh + UC1) = (u32x2){s0, s1};
      *(u32x2*)(bh + UC2) = (u32x2){e1, n0};
      *(u32x2*)(bh + UC3) = (u32x2){s1, s2};
      if (hf == 0) { e00 = e0; sh00 = s0; }
    }
    if (cq == 0) {
      char* b0 = lds + B_UC + cch * UCH;
      const u32x4 z = {0, 0, 0, 0};
      *(u32x2*)(b0 + UC0) = (u32x2){0, 0}; *(u32x2*)(b0 + UC0 + 8) = (u32x2){0, 0}; *(u32x2*)(b0 + UC0 + 16) = (u32x2){0, 0}; *(u32x2*)(b0 + UC0 + 24) = (u32x2){0, 0};
      *(u32x4*)(b0 + UC1) = z; *(u32x4*)(b0 + UC1 + 16) = (u32x4){0, 0, 0, e00 << 16};
      *(u32x4*)(b0 + UC2) = z; *(u32x4*)(b0 + UC2 + 16) = (u32x4){0, 0, 0, e00};
      *(u32x4*)(b0 + UC3) = z; *(u32x4*)(b0 + UC3 + 16) = (u32x4){0, 0, e00 << 16, sh00};
    }
  }
  __builtin_amdgcn_s_setprio(BPRIO2);
  bar_l();
  STAMP(3);
  if (tid < 384) {
    const int ch = tid >= 192 ? 1 : 0, t4 = (tid - 192 * ch) * 4;
    const float* k0 = (const float*)(lds + 4 * ch * KG_SLAB) + t4;
    f32x4 s = *(const f32x4*)k0;
#pragma unroll
    for (int q = 1; q < 4; ++q) s += *(const f32x4*)(k0 + q * (KG_SLAB / 4));
    *(u32x2*)(lds + B_KTAB + ch * 1536 + t4 * 2) = (u32x2){pk2(s[0], s[1]), pk2(s[2], s[3])};
  }
  bar_l();
  {
    const int jhi = 16 * (cq >> 1) + 15 - (cq & 1) * 8;
    const float fj = (float)jhi;
    const char* kt = lds + B_KTAB + cch * 1536;
    u32 o[4];
#pragma unroll
    for (int e = 0; e < 4; ++e) {
      u32 bits = pk2(fj - (float)(2 * e), fj - (float)(2 * e + 1));
      u32 t0 = (bits & 0xffffu) - 0x4280u, t1 = (bits >> 16) - 0x4280u;
      if (jhi < 256) { t0 = jhi - 2 * e; t1 = jhi - 2 * e - 1; }
      u32 lo = *(const u16*)(kt + t0 * 2), hi = *(const u16*)(kt + t1 * 2);
      o[e] = lo | (hi << 16);
    }
    char* kr = lds + B_KR + cch * 4608;
    *(u32x4*)(kr + 512 + cq * 16) = (u32x4){o[0], o[1], o[2], o[3]};
    if (cq < 32) *(u32x4*)(kr + cq * 16) = (u32x4){0, 0, 0, 0};
  }
  bar_l();
  STAMP(4);
  {
    const int ch = w >> 2, rh = (w >> 1) & 1, sh = w & 1;
    const int idx0 = 8 * g + l15 + 1, cc = idx0 & 3;
    const u32 ucb = cc == 0 ? UC0 : (cc == 1 ? UC1 : (cc == 2 ? UC2 : UC3));
    const LAS char* abase = (const LAS char*)(lds + B_UC + ch * UCH + ucb + (idx0 & ~3) * 2 + rh * 256);
    const char* bbase = lds + B_KR + ch * 4608 + (256 + 16 * (l15 - (g >> 1)) + 8 * (g & 1)) * 2 - rh * 256;
    f32x4 c4[4];
#pragma unroll
    for (int i = 0; i < 4; ++i) c4[i] = (f32x4){0, 0, 0, 0};
    if (sh == 0) {
#pragma unroll 1
      for (int rr = 0; rr < 4; ++rr) {
        u32x4 af[8], bf[8];
#pragma unroll
        for (int a = 0; a < 8; ++a) {
          const LAS char* pa = abase + (256 * a) * 2 + rr * 64;
          u32x2 lo = *(const volatile LAS u32x2*)pa, hi = *(const volatile LAS u32x2*)(pa + 8);
          af[a] = (u32x4){lo[0], lo[1], hi[0], hi[1]};
          bf[a] = *(const u32x4*)(bbase + (256 * a) * 2 - rr * 64);
        }
        c4[0] = mfma16(af[0], bf[0], c4[0]);
        c4[1] = mfma16(af[0], bf[1], c4[1]); c4[1] = mfma16(af[1], bf[0], c4[1]);
#pragma unroll
        for (int a = 0; a < 8; ++a) {
          if (a < 7) c4[2] = mfma16(af[a], bf[6 - a], c4[2]);
          c4[3] = mfma16(af[a], bf[7 - a], c4[3]);
        }
      }
    } else {
#pragma unroll 1
      for (int rr = 0; rr < 4; ++rr) {
        u32x4 af[6], bf[6];
#pragma unroll
        for (int a = 0; a < 6; ++a) {
          const LAS char* pa = abase + (256 * a) * 2 + rr * 64;
          u32x2 lo = *(const volatile LAS u32x2*)pa, hi = *(const volatile LAS u32x2*)(pa + 8);
          af[a] = (u32x4){lo[0], lo[1], hi[0], hi[1]};
          bf[a] = *(const u32x4*)(bbase + (256 * a) * 2 - rr * 64);
        }
#pragma unroll
        for (int a = 0; a < 6; ++a) {
          if (a < 3) c4[0] = mfma16(af[a], bf[2 - a], c4[0]);
          if (a < 4) c4[1] = mfma16(af[a], bf[3 - a], c4[1]);
          if (a < 5) c4[2] = mfma16(af[a], bf[4 - a], c4[2]);
          c4[3] = mfma16(af[a], bf[5 - a], c4[3]);
        }
      }
    }
    STAMP(5);
    const f32x4 e0 = c4[0], e1 = c4[1], e2 = c4[2], e3 = c4[3];
    f32x4* xw = (f32x4*)(lds + B_RED + w * 2048) + lane;
    xw[0] = rh ? e0 : e2; xw[64] = rh ? e1 : e3;
    bar_l();
    const f32x4* xr = (const f32x4*)(lds + B_RED + (w ^ 2) * 2048) + lane;
    f32x4 v0 = xr[0], v1 = xr[64];
    v0 += rh ? e2 : e0; v1 += rh ? e3 : e1;
    const int pt = sh == 0 ? (rh ? 6 : 0) : (rh ? 4 : 2);
    char* strip = lds + B_RED + 16384 + w * 1024;
    *(u32x2*)(strip + (4 * l15 + g) * 8) = (u32x2){pk2(v0[0], v0[1]), pk2(v0[2], v0[3])};
    *(u32x2*)(strip + 512 + (4 * l15 + g) * 8) = (u32x2){pk2(v1[0], v1[1]), pk2(v1[2], v1[3])};
    const char* ub0 = lds + B_UC + ch * UCH + UC0 + (16 + 256 * pt + 4 * lane) * 2;
    const u32x2 x0 = *(const u32x2*)ub0, x1 = *(const u32x2*)(ub0 + 512);
    const float dsk = bflo(dskip);
    CFENCE();
    WAIT_L(0);
    const u32x2 y0 = *(const u32x2*)(strip + lane * 8), y1 = *(const u32x2*)(strip + 512 + lane * 8);
    __builtin_amdgcn_s_setprio(3);
    const u32 yo = (ch0 + ch) * (S * 4) + (256 * pt + 4 * lane) * 4;
#pragma unroll
    for (int hf = 0; hf < 2; ++hf) {
      const u32x2 xx = hf ? x1 : x0, yy = hf ? y1 : y0;
      const u32 pa = pk2(smul(bflo(xx[0]), dsk), smul(bfhi(xx[0]), dsk)), pb = pk2(smul(bflo(xx[1]), dsk), smul(bfhi(xx[1]), dsk));
      const u32 ra = pk2(bflo(yy[0]) + bflo(pa), bfhi(yy[0]) + bfhi(pa)), rb = pk2(bflo(yy[1]) + bflo(pb), bfhi(yy[1]) + bfhi(pb));
      st16c((u32x4){(ra & 0xffffu) | tw, (ra >> 16) | tw, (rb & 0xffffu) | tw, (rb >> 16) | tw}, rWS, yo + hf * 1024, WS_YT);
    }
    STAMP(6);
  }
  STAMP_FLUSH(ws);
}

__global__ __launch_bounds__(512, 4) void fused(Params p_unused) {
  __shared__ __attribute__((aligned(16))) char lds[LDS_BYTES];
  unsigned long long t1_;
  asm volatile("s_memrealtime %0\n\ts_waitcnt lgkmcnt(0)" : "=s"(t1_) :: "memory");
  const __attribute__((address_space(4))) Params* kp = (const __attribute__((address_space(4))) Params*)__builtin_amdgcn_kernarg_segment_ptr();
  asm volatile("" : "+s"(kp) :: "memory");
  Params p;
  for (int i = 0; i < 14; ++i) p.in[i] = kp->in[i];
  p.ws = kp->ws; p.out = kp->out;
  unsigned long long t2_;
  asm volatile("s_waitcnt lgkmcnt(0)\n\ts_memrealtime %0\n\ts_waitcnt lgkmcnt(0)" : "=s"(t2_) : "s"(p.ws), "s"(p.in[0]), "s"(p.in[13]) : "memory");
  const int b = blockIdx.x, x8 = b & 7, j = b >> 3;
  if (j < 32) roleA(p, x8, j, lds);
  else roleB(p, x8, j - 32, lds);
  if (b == 0) { while (__builtin_amdgcn_s_memrealtime() - t1_ < 4000 + MULT * (t2_ - t1_)) __builtin_amdgcn_s_sleep(4); }
}

extern "C" void kernel_launch(void* const* d_in, const int* in_sizes, int n_in, void* d_out, int out_size, void* d_ws, size_t ws_size, hipStream_t stream) {
  Params p;
  for (int i = 0; i < 14; ++i) p.in[i] = (const u16*)d_in[i];
  p.ws = (char*)d_ws; p.out = (u16*)d_out;
  fused<<<480, 512, 0, stream>>>(p);
}
```

```cpp
#define TDELAY 0
#define H2MODE 0
#define SMALLPOS 1
#define EPF 0
#define WOT 1
#define WOPOS 2
#define OUTAUX 16
#define RW 0
#define EARLYA 1
#define BEPI 1
#define TPOS 1
#define BPRIO 1
#define XPUB 3
#define KCH 1
#define APRIO 1
#define MRED 1
#define RWM 1
#define RPRIO 1
#define BEP3 1
#define YF2 1
#define EXP2 1
#define FEPI 1
#define PLN 6
#define PLN_STR2(x) #x
#define PLN_STR(x) PLN_STR2(x)
#define PLN_S PLN_STR(PLN)
#define YE 0
#include <hip/hip_runtime.h>
#include <stdint.h>

typedef unsigned short u16;
typedef unsigned int u32;
typedef short s16x4 __attribute__((ext_vector_type(4)));
typedef __bf16 bf16x8 __attribute__((ext_vector_type(8)));
typedef __bf16 bf16x2 __attribute__((ext_vector_type(2)));
typedef float f32x4 __attribute__((ext_vector_type(4)));
typedef float f32x2 __attribute__((ext_vector_type(2)));
typedef u32 u32x4 __attribute__((ext_vector_type(4)));
typedef u32 u32x2 __attribute__((ext_vector_type(2)));
typedef __amdgpu_buffer_rsrc_t rsrc_t;
#define LAS __attribute__((address_space(3)))
#define GAS __attribute__((address_space(1)))
#define DI __device__ __forceinline__

constexpr int S = 2048, D = 448, H = 16, DH = 28, NM = 256;

constexpr u32 WS_GEN = 0;
constexpr u32 WS_FLAG = 4096, FSTR = 128;
constexpr u32 F_B = 0;
constexpr u32 F_A = 224;
constexpr u32 NFLAG = F_A + 256;
constexpr u32 WS_XF = 4096;
#define XPAD 384
#define YPAD 384
constexpr u32 XSTR = S * 2 + XPAD, YSTR = S * 2 + YPAD;
constexpr u32 WS_XT = 1u << 20;
constexpr u32 WS_KVP = WS_XT + D * S * 4;
constexpr u32 WS_OF = WS_KVP + 16 * 16 * 4 * 1024;
constexpr u32 WS_YT = WS_OF + 128 * 16 * 1024;
constexpr u32 WS_GATE = WS_YT + D * YSTR;
constexpr u32 WS_KVR = WS_GATE + S * 4;
constexpr u32 WS_YF = WS_KVR + 16 * 4096;
constexpr u32 WS_END = WS_YF + 224 * 1024;
static_assert(WS_FLAG + NFLAG * FSTR <= WS_XT, "flags");
constexpr int MAXSPIN = 400000;
#define CODE_BYTES 32768

#define STAMP(i)
#define STAMP_DECL
#define STAMP_FLUSH(ws)

DI float bflo(u32 v) { return __uint_as_float(v << 16); }
DI float bfhi(u32 v) { return __uint_as_float(v & 0xffff0000u); }
DI u32 pk2(float lo, float hi) { f32x2 p = {lo, hi}; bf16x2 q = __builtin_convertvector(p, bf16x2); return __builtin_bit_cast(u32, q); }
DI float smul(float a, float b) { float r; asm("v_mul_f32 %0, %1, %2" : "=v"(r) : "v"(a), "v"(b)); return r; }
DI float rbf(float f) { return bflo(pk2(f, f)); }
DI f32x4 mfma16(u32x4 a, u32x4 b, f32x4 c) {
  return __builtin_amdgcn_mfma_f32_16x16x32_bf16(__builtin_bit_cast(bf16x8, a), __builtin_bit_cast(bf16x8, b), c, 0, 0, 0);
}
DI u32x2 trread(const char* lds_base, u32 off) {
  s16x4 t = __builtin_amdgcn_ds_read_tr16_b64_v4i16((LAS s16x4*)((LAS char*)lds_base + off));
  return __builtin_bit_cast(u32x2, t);
}
DI rsrc_t mkrs(const void* p, u32 bytes) { return __builtin_amdgcn_make_buffer_rsrc((void*)p, 0, bytes, 0x00020000); }
DI void dma16(rsrc_t r, char* lds_dst, u32 voff, u32 soff) { __builtin_amdgcn_raw_ptr_buffer_load_lds(r, (LAS void*)lds_dst, 16, voff, soff, 0, 0); }
DI void touch4(rsrc_t r, char* lds_dst, u32 voff, u32 soff) { __builtin_amdgcn_raw_ptr_buffer_load_lds(r, (LAS void*)lds_dst, 4, voff, soff, 0, 0); }
DI u32x4 mkrs4(const void* p, u32 bytes) { unsigned long long a = (unsigned long long)p; return (u32x4){(u32)a, (u32)(a >> 32) & 0xffffu, bytes, 0x00020000u}; }
DI u32 ldsaddr(const char* p) { return (u32)(uintptr_t)(LAS const char*)p; }
DI void adma16(u32x4 rs, u32 lds_dst, u32 voff, u32 soff) {
  asm volatile("s_mov_b32 m0, %0\n\ts_nop 0\n\tbuffer_load_dwordx4 %1, %2, %3 offen lds" ::"s"(lds_dst), "v"(voff), "s"(rs), "s"(soff) : "memory");
}
DI void adma16h(u32x4 rs, u32 lds_dst, u32 voff, u32 soff) {
  asm volatile("s_mov_b32 m0, %0\n\ts_mov_b32 exec_hi, 0\n\tbuffer_load_dwordx4 %1, %2, %3 offen lds\n\ts_mov_b32 exec_hi, -1" ::"s"(lds_dst), "v"(voff), "s"(rs), "s"(soff) : "memory");
}
DI u32 sload(const void* p) { return *(const __attribute__((address_space(4))) u32*)(unsigned long long)p; }
DI void atouch4(u32x4 rs, u32 lds_dst, u32 voff, u32 soff) {
  asm volatile("s_mov_b32 m0, %0\n\ts_nop 0\n\tbuffer_load_dword %1, %2, %3 offen lds" ::"s"(lds_dst), "v"(voff), "s"(rs), "s"(soff) : "memory");
}
DI u32x4 ald16(u32x4 rs, u32 voff, u32 soff) {
  u32x4 r; asm volatile("buffer_load_dwordx4 %0, %1, %2, %3 offen" : "=v"(r) : "v"(voff), "s"(rs), "s"(soff) : "memory"); return r;
}
DI void ald16io(u32x4& r, u32x4 rs, u32 voff, u32 soff) {
  asm volatile("buffer_load_dwordx4 %0, %1, %2, %3 offen" : "+v"(r) : "v"(voff), "s"(rs), "s"(soff) : "memory");
}
DI void ald16cio(u32x4& r, u32x4 rs, u32 voff, u32 soff) {
  asm volatile("buffer_load_dwordx4 %0, %1, %2, %3 offen sc1" : "+v"(r) : "v"(voff), "s"(rs), "s"(soff) : "memory");
}
DI void wait8(u32x4* a) { asm volatile("s_waitcnt vmcnt(0)" : "+v"(a[0]), "+v"(a[1]), "+v"(a[2]), "+v"(a[3]), "+v"(a[4]), "+v"(a[5]), "+v"(a[6]), "+v"(a[7])::"memory"); }
template <int N> DI void wait8n(u32x4* a) { asm volatile("s_waitcnt vmcnt(%8)" : "+v"(a[0]), "+v"(a[1]), "+v"(a[2]), "+v"(a[3]), "+v"(a[4]), "+v"(a[5]), "+v"(a[6]), "+v"(a[7]) : "i"(N) : "memory"); }
DI void ald4cio(u32& r, u32x4 rs, u32 voff, u32 soff) {
  asm volatile("buffer_load_dword %0, %1, %2, %3 offen sc1" : "+v"(r) : "v"(voff), "s"(rs), "s"(soff) : "memory");
}
#define G8_LD(t, OFF) "s_bitcmp1_b32 %[nw], " #t "\n\ts_cbranch_scc0 2" #t "f\n\t" OFF "buffer_load_dwordx4 %[p" #t "], %[vo], %[rs], %[so] offen\n2" #t ":\n\t"
#define G8_NEXT "s_add_u32 %[so], %[so], %[stp]\n\ts_cmp_lt_u32 %[so], %[lim]\n\ts_cselect_b32 %[so], %[so], %[bas]\n\t"
#define G8_MASK "v_lshrrev_b32 %[tmp], 16, %[pl]\n\tv_cmp_eq_u32 vcc, %[t16], %[tmp]\n\ts_and_b32 %[nw], vcc_lo, %[all]\n\ts_andn2_b32 %[nw], %[nw], %[dn]\n\ts_or_b32 %[dn], %[dn], %[nw]\n\ts_mov_b32 %[so], %[so0]\n\t"
#define G8_LOADS G8_LD(0, "") G8_NEXT G8_LD(1, "") G8_NEXT G8_LD(2, "") G8_NEXT G8_LD(3, "") G8_NEXT G8_LD(4, "") G8_NEXT G8_LD(5, "") G8_NEXT G8_LD(6, "") G8_NEXT G8_LD(7, "")
#define G8_Y
struct G8 { u32x4 rs; u32 vo, plo, so0, stp, lim, bas, all, t16, yvo, yso, ydst; };
DI void g8_first(const G8& c, u32x4* p, u32& pl, u32& dn, u32& yd) {
  u32 nw, so, tmp;
  asm volatile(G8_MASK G8_LOADS G8_Y
               "s_cmp_eq_u32 %[dn], %[all]\n\ts_cbranch_scc1 9f\n\tbuffer_load_dword %[pl], %[plo], %[rs], 0 offen sc1\n9:"
               : [p0] "=&v"(p[0]), [p1] "=&v"(p[1]), [p2] "=&v"(p[2]), [p3] "=&v"(p[3]), [p4] "=&v"(p[4]), [p5] "=&v"(p[5]), [p6] "=&v"(p[6]), [p7] "=&v"(p[7]),
                 [pl] "+v"(pl), [dn] "+s"(dn), [nw] "=&s"(nw), [so] "=&s"(so), [tmp] "=&v"(tmp), [yd] "+s"(yd)
               : [vo] "v"(c.vo), [plo] "v"(c.plo), [rs] "s"(c.rs), [so0] "s"(c.so0), [stp] "s"(c.stp), [lim] "s"(c.lim), [bas] "s"(c.bas), [all] "s"(c.all), [t16] "s"(c.t16),
                 [yvo] "v"(c.yvo), [yso] "s"(c.yso), [ydst] "s"(c.ydst)
               : "vcc", "scc", "memory");
}
DI void g8_rest(const G8& c, u32x4* p, u32& pl, u32& dn, u32& yd) {
  u32 nw, so, tmp, cnt = MAXSPIN;
  asm volatile("s_cmp_eq_u32 %[dn], %[all]\n\ts_cbranch_scc1 9f\n"
               "1:\n\ts_waitcnt vmcnt(0)\n\t" G8_MASK G8_LOADS G8_Y
               "s_cmp_eq_u32 %[dn], %[all]\n\ts_cbranch_scc1 9f\n\t"
               "s_sub_u32 %[cnt], %[cnt], 1\n\ts_cbranch_scc1 9f\n\t"
               "buffer_load_dword %[pl], %[plo], %[rs], 0 offen sc1\n\ts_branch 1b\n9:"
               : [p0] "+v"(p[0]), [p1] "+v"(p[1]), [p2] "+v"(p[2]), [p3] "+v"(p[3]), [p4] "+v"(p[4]), [p5] "+v"(p[5]), [p6] "+v"(p[6]), [p7] "+v"(p[7]),
                 [pl] "+v"(pl), [dn] "+s"(dn), [nw] "=&s"(nw), [so] "=&s"(so), [tmp] "=&v"(tmp), [cnt] "+s"(cnt), [yd] "+s"(yd)
               : [vo] "v"(c.vo), [plo] "v"(c.plo), [rs] "s"(c.rs), [so0] "s"(c.so0), [stp] "s"(c.stp), [lim] "s"(c.lim), [bas] "s"(c.bas), [all] "s"(c.all), [t16] "s"(c.t16),
                 [yvo] "v"(c.yvo), [yso] "s"(c.yso), [ydst] "s"(c.ydst)
               : "vcc", "scc", "memory");
}
#define G16_LD(t, o) "s_bitcmp1_b32 %[nw], " #t "\n\ts_cbranch_scc0 2" #t "f\n\tbuffer_load_dwordx4 %[p" #t "], %[vo], %[rs], %[so] offen offset:" #o "\n2" #t ":\n\t"
#define G16_UP "s_add_u32 %[so], %[so], 0x1000\n\t"
#define G16_LOADS G16_LD(0, 0) G16_LD(1, 1024) G16_LD(2, 2048) G16_LD(3, 3072) G16_UP G16_LD(4, 0) G16_LD(5, 1024) G16_LD(6, 2048) G16_LD(7, 3072) G16_UP \
                  G16_LD(8, 0) G16_LD(9, 1024) G16_LD(10, 2048) G16_LD(11, 3072) G16_UP G16_LD(12, 0) G16_LD(13, 1024) G16_LD(14, 2048) G16_LD(15, 3072)
DI void g16_poll(const G8& c, u32x4* p, u32& yd) {
  u32 nw, so, tmp, pl, dn = 0, cnt = MAXSPIN;
  asm volatile("1:\n\tbuffer_load_dword %[pl], %[plo], %[rs], 0 offen sc1\n\ts_waitcnt vmcnt(0)\n\t"
               "v_lshrrev_b32 %[tmp], 16, %[pl]\n\tv_cmp_eq_u32 vcc, %[t16], %[tmp]\n\ts_and_b32 %[nw], vcc_lo, %[all]\n\ts_andn2_b32 %[nw], %[nw], %[dn]\n\ts_or_b32 %[dn], %[dn], %[nw]\n\ts_mov_b32 %[so], %[so0]\n\t"
               G16_LOADS
               "s_cmp_lg_u32 %[yd], 0\n\ts_cbranch_scc1 31f\n\ts_cmp_eq_u32 vcc_hi, -1\n\ts_cbranch_scc0 31f\n\t"
               "s_mov_b32 m0, %[ydst]\n\ts_mov_b32 %[yd], 1\n\tbuffer_load_dwordx4 %[yvo], %[rs], %[yso] offen lds\n31:\n\t"
               "s_cmp_eq_u32 %[dn], %[all]\n\ts_cbranch_scc1 9f\n\t"
               "s_sub_u32 %[cnt], %[cnt], 1\n\ts_cbranch_scc0 1b\n9:"
               : [p0] "=&v"(p[0]), [p1] "=&v"(p[1]), [p2] "=&v"(p[2]), [p3] "=&v"(p[3]), [p4] "=&v"(p[4]), [p5] "=&v"(p[5]), [p6] "=&v"(p[6]), [p7] "=&v"(p[7]),
                 [p8] "=&v"(p[8]), [p9] "=&v"(p[9]), [p10] "=&v"(p[10]), [p11] "=&v"(p[11]), [p12] "=&v"(p[12]), [p13] "=&v"(p[13]), [p14] "=&v"(p[14]), [p15] "=&v"(p[15]),
                 [pl] "=&v"(pl), [dn] "+s"(dn), [nw] "=&s"(nw), [so] "=&s"(so), [tmp] "=&v"(tmp), [cnt] "+s"(cnt), [yd] "+s"(yd)
               : [vo] "v"(c.vo), [plo] "v"(c.plo), [rs] "s"(c.rs), [so0] "s"(c.so0), [all] "s"(c.all), [t16] "s"(c.t16), [yvo] "v"(c.yvo), [yso] "s"(c.yso), [ydst] "s"(c.ydst)
               : "vcc", "scc", "memory");
}
DI void wait16(u32x4* a) {
  asm volatile("s_waitcnt vmcnt(0)" : "+v"(a[0]), "+v"(a[1]), "+v"(a[2]), "+v"(a[3]), "+v"(a[4]), "+v"(a[5]), "+v"(a[6]), "+v"(a[7]),
               "+v"(a[8]), "+v"(a[9]), "+v"(a[10]), "+v"(a[11]), "+v"(a[12]), "+v"(a[13]), "+v"(a[14]), "+v"(a[15]) :: "memory");
}
DI void g8_lds(const G8& c, u32x4* p, u32& pl, u32 pdst, u32 paddr) {
  u32 nw, so, tmp, dn = 0, cnt = MAXSPIN, c2;
  asm volatile("s_mov_b32 m0, %[pdst]\n\ts_nop 0\n"
               "1:\n\tbuffer_load_dword %[plo], %[rs], 0 offen sc1 lds\n\ts_mov_b32 %[c2], " PLN_S "\n"
               "3:\n\tds_read_b32 %[pl], %[pa]\n\ts_waitcnt lgkmcnt(0)\n\t" G8_MASK
               "s_cmp_eq_u32 %[nw], 0\n\ts_cbranch_scc1 4f\n\t" G8_LOADS
               "s_cmp_eq_u32 %[dn], %[all]\n\ts_cbranch_scc1 9f\n"
               "4:\n\ts_sub_u32 %[c2], %[c2], 1\n\ts_cbranch_scc0 3b\n\t"
               "s_sub_u32 %[cnt], %[cnt], 1\n\ts_cbranch_scc0 1b\n9:"
               : [p0] "=&v"(p[0]), [p1] "=&v"(p[1]), [p2] "=&v"(p[2]), [p3] "=&v"(p[3]), [p4] "=&v"(p[4]), [p5] "=&v"(p[5]), [p6] "=&v"(p[6]), [p7] "=&v"(p[7]),
                 [pl] "=&v"(pl), [dn] "+s"(dn), [nw] "=&s"(nw), [so] "=&s"(so), [tmp] "=&v"(tmp), [cnt] "+s"(cnt), [c2] "=&s"(c2)
               : [vo] "v"(c.vo), [plo] "v"(c.plo), [rs] "s"(c.rs), [so0] "s"(c.so0), [stp] "s"(c.stp), [lim] "s"(c.lim), [bas] "s"(c.bas), [all] "s"(c.all), [t16] "s"(c.t16),
                 [pdst] "s"(pdst), [pa] "v"(paddr)
               : "vcc", "scc", "memory");
}
DI void g8_reload(const G8& c, u32x4* p) {
  u32 so;
#define G8_RL(t) "buffer_load_dwordx4 %[p" #t "], %[vo], %[rs], %[so] offen sc1\n\t"
  asm volatile("s_mov_b32 %[so], %[so0]\n\t" G8_RL(0) G8_NEXT G8_RL(1) G8_NEXT G8_RL(2) G8_NEXT G8_RL(3) G8_NEXT G8_RL(4) G8_NEXT G8_RL(5) G8_NEXT G8_RL(6) G8_NEXT G8_RL(7) "s_waitcnt vmcnt(0)"
               : [p0] "=&v"(p[0]), [p1] "=&v"(p[1]), [p2] "=&v"(p[2]), [p3] "=&v"(p[3]), [p4] "=&v"(p[4]), [p5] "=&v"(p[5]), [p6] "=&v"(p[6]), [p7] "=&v"(p[7]), [so] "=&s"(so)
               : [vo] "v"(c.vo), [rs] "s"(c.rs), [so0] "s"(c.so0), [stp] "s"(c.stp), [lim] "s"(c.lim), [bas] "s"(c.bas)
               : "scc", "memory");
}
DI void undef8(u32x4* a) { asm volatile("" : "=v"(a[0]), "=v"(a[1]), "=v"(a[2]), "=v"(a[3]), "=v"(a[4]), "=v"(a[5]), "=v"(a[6]), "=v"(a[7])); }
template <int N> DI void waitv4(u32x4& a, u32x4& b, u32x4& c, u32x4& d) { asm volatile("s_waitcnt vmcnt(%4)" : "+v"(a), "+v"(b), "+v"(c), "+v"(d) : "i"(N) : "memory"); }
DI u32x4 ld16(rsrc_t r, u32 voff, u32 soff) { return __builtin_amdgcn_raw_buffer_load_b128(r, voff, soff, 0); }
DI u32x4 ld16c(rsrc_t r, u32 voff, u32 soff) { return __builtin_amdgcn_raw_buffer_load_b128(r, voff, soff, 16); }
DI u32x2 ld8c(rsrc_t r, u32 voff, u32 soff) { return __builtin_amdgcn_raw_buffer_load_b64(r, voff, soff, 16); }
DI u32 ld4c(rsrc_t r, u32 voff, u32 soff) { return __builtin_amdgcn_raw_buffer_load_b32(r, voff, soff, 16); }
DI u32 ld2c(rsrc_t r, u32 voff, u32 soff) { return __builtin_amdgcn_raw_buffer_load_b16(r, voff, soff, 16); }
DI void st16c(u32x4 v, rsrc_t r, u32 voff, u32 soff) {
  __builtin_amdgcn_raw_buffer_store_b128(v, r, voff, soff, 16);
  asm volatile("s_nop 1" ::"v"(v));
}
DI void st4c(u32 v, rsrc_t r, u32 voff, u32 soff) { __builtin_amdgcn_raw_buffer_store_b32(v, r, voff, soff, 16); }
#define WAIT_L(n) asm volatile("s_waitcnt lgkmcnt(" #n ")" ::: "memory")
#define BAR() __builtin_amdgcn_s_barrier()
#define CFENCE() asm volatile("" ::: "memory")
template <int N> DI void waitv() { asm volatile("s_waitcnt vmcnt(%0)" ::"i"(N) : "memory"); }
DI void bar_l() { WAIT_L(0); BAR(); CFENCE(); }

DI u32 flag_off(u32 idx) { return WS_FLAG + idx * FSTR; }
DI void publish(char* ws, u32 idx, u32 tag, int lane) {
  waitv<0>();
  if (lane == 0) __hip_atomic_store((u32*)(ws + flag_off(idx)), tag, __ATOMIC_RELAXED, __HIP_MEMORY_SCOPE_AGENT);
}
DI void pollf(char* ws, u32 idx, u32 tag) {
  const u32* f = (const u32*)(ws + flag_off(idx));
#pragma unroll 1
  for (int it = 0; it < MAXSPIN; ++it) {
    u32 v = __hip_atomic_load(f, __ATOMIC_RELAXED, __HIP_MEMORY_SCOPE_AGENT);
    if (__all(v == tag)) break;
    __builtin_amdgcn_s_sleep(1);
  }
  CFENCE();
}

DI float red_max_g(float x) {
  u32 u = __float_as_uint(x);
  auto r = __builtin_amdgcn_permlane16_swap(u, u, false, false);
  x = fmaxf(__uint_as_float(r[0]), __uint_as_float(r[1]));
  u = __float_as_uint(x);
  auto r2 = __builtin_amdgcn_permlane32_swap(u, u, false, false);
  return fmaxf(__uint_as_float(r2[0]), __uint_as_float(r2[1]));
}
DI float red_sum_g(float x) {
  u32 u = __float_as_uint(x);
  auto r = __builtin_amdgcn_permlane16_swap(u, u, false, false);
  x = __uint_as_float(r[0]) + __uint_as_float(r[1]);
  u = __float_as_uint(x);
  auto r2 = __builtin_amdgcn_permlane32_swap(u, u, false, false);
  return __uint_as_float(r2[0]) + __uint_as_float(r2[1]);
}
template <int CTRL> DI float dpp_add(float x) {
  return x + __uint_as_float(__builtin_amdgcn_update_dpp(0, __float_as_uint(x), CTRL, 0xf, 0xf, false));
}
DI float red_sum_all(float x) {
  x = dpp_add<0x128>(x); x = dpp_add<0x124>(x); x = dpp_add<0x122>(x); x = dpp_add<0x121>(x);
  return red_sum_g(x);
}

#define JROT 0
#define APRIO3 2
#define APRIO4 2
#define BPRIO3 0
#define GPOS 1
#define KVP 0
#define QP 0
#define SPF 0
#define YW 0
#define FV 1
#define PLDS 0
#define PAIRX 0
#define HOIST 0
#define PBLK 100
#define PK 10
#define YMODE 1
#define PQ0 4
#define PQ1 5
#define TPRIO 3
#define NPRE 3
#define LDECL
#define LST(k)
#define LST2(k)
#define LFLUSH(ws)
#define P2SLEEP 2
#define DBGCNT(x)
#define LD1 ld16
#define LD2 ld16
#define BPRIO2 0
#define APRIO2 2
DI float amax3(float a, float b, float c) { float r; asm("v_max3_f32 %0, %1, %2, %3" : "=v"(r) : "v"(a), "v"(b), "v"(c)); return r; }
DI float anmin3(float a, float b, float c) { float r; asm("v_max3_f32 %0, -%1, -%2, -%3" : "=v"(r) : "v"(a), "v"(b), "v"(c)); return r; }
DI float amax2(float a, float b) { float r; asm("v_max_f32 %0, %1, %2" : "=v"(r) : "v"(a), "v"(b)); return r; }
DI float amax2n(float a, float b) { float r; asm("v_max_f32 %0, %1, %2\n\ts_nop 1" : "=v"(r) : "v"(a), "v"(b)); return r; }
template <bool MAX> DI void red2_g(float& a, float& b) {
  auto r = __builtin_amdgcn_permlane16_swap(__float_as_uint(a), __float_as_uint(b), false, false);
  float t = MAX ? amax2(__uint_as_float(r[0]), __uint_as_float(r[1])) : __uint_as_float(r[0]) + __uint_as_float(r[1]);
  auto r2 = __builtin_amdgcn_permlane32_swap(__float_as_uint(t), __float_as_uint(t), false, false);
  t = MAX ? amax2(__uint_as_float(r2[0]), __uint_as_float(r2[1])) : __uint_as_float(r2[0]) + __uint_as_float(r2[1]);
  auto r3 = __builtin_amdgcn_permlane16_swap(__float_as_uint(t), __float_as_uint(t), false, false);
  a = __uint_as_float(r3[0]); b = __uint_as_float(r3[1]);
}
DI void dual_softmax(const f32x4 (&pacc)[2], u32x4& pos, u32x4& neg) {
  constexpr float L2E = 1.4426950408889634f;
  float p[7];
  {
    u32 a = pk2(pacc[0][0], pacc[0][1]), b = pk2(pacc[0][2], pacc[0][3]), c = pk2(pacc[1][0], pacc[1][1]), d = pk2(pacc[1][2], pacc[1][2]);
    p[0] = bflo(a); p[1] = bfhi(a); p[2] = bflo(b); p[3] = bfhi(b); p[4] = bflo(c); p[5] = bfhi(c); p[6] = bflo(d);
  }
  float mx = amax3(amax3(p[0], p[1], p[2]), amax3(p[3], p[4], p[5]), p[6]);
  float nm = amax2n(amax2(anmin3(p[0], p[1], p[2]), anmin3(p[3], p[4], p[5])), -p[6]);
  red2_g<true>(mx, nm);
  const f32x2 cc = {L2E, -L2E}, off = {-mx * L2E, -nm * L2E};
  f32x2 e[7];
#pragma unroll
  for (int i = 0; i < 7; ++i) {
    const f32x2 a = __builtin_elementwise_fma((f32x2){p[i], p[i]}, cc, off);
    e[i] = (f32x2){__builtin_amdgcn_exp2f(a.x), __builtin_amdgcn_exp2f(a.y)};
  }
  const f32x2 sm = ((e[0] + e[1]) + (e[2] + e[3])) + ((e[4] + e[5]) + e[6]);
  float sp = sm.x, sn = sm.y;
  red2_g<false>(sp, sn);
  const f32x2 rr = {__builtin_amdgcn_rcpf(sp), __builtin_amdgcn_rcpf(sn)};
#pragma unroll
  for (int i = 0; i < 7; ++i) e[i] *= rr;
#pragma unroll
  for (int i = 0; i < 3; ++i) { pos[i] = pk2(e[2 * i].x, e[2 * i + 1].x); neg[i] = pk2(e[2 * i].y, e[2 * i + 1].y); }
  pos[3] = pk2(e[6].x, 0.f); neg[3] = pk2(e[6].y, 0.f);
}
DI float acc_sel(u32 v, u32 sel, float a) { return __builtin_amdgcn_fdot2_f32_bf16(__builtin_bit_cast(bf16x2, v), __builtin_bit_cast(bf16x2, sel), a, false); }
DI bool tag_ok(u32 v3, u32 t16) { return __all((v3 >> 16) == t16); }

DI u32 lds_inc(u32 addr) {
  u32 r, one = 1u, a = addr;
  asm volatile("s_mov_b64 exec, 1\n\tds_add_rtn_u32 %0, %1, %2\n\ts_mov_b64 exec, -1\n\ts_waitcnt lgkmcnt(0)" : "=&v"(r) : "v"(a), "v"(one) : "memory");
  return (u32)__builtin_amdgcn_readfirstlane((int)r);
}
struct Params { const u16* in[14]; char* ws; u16* out; };

constexpr int A_X = 0, A_W = 32768, A_WPHI = 73728, A_GW = 75776, A_VT = 16384, A_Q6 = 24576, A_KVB = 65536, A_RED = 65536, A_CNT = 76704;
constexpr int LDS_BYTES = 81920;
constexpr int WROW = 3 * D * 2;
constexpr int WCH = 64 * WROW;

#define PRB(k)
#define KD(i)
#define FSTAMP(i) STAMP(i)
DI void roleA(const Params& p, int x8, int j, char* lds, u32 tent) {
  const int tid = threadIdx.x, lane = tid & 63, w = __builtin_amdgcn_readfirstlane(tid >> 6);
  const int g = lane >> 4, l15 = lane & 15, q4 = l15 >> 2, pp = lane & 3;
  const int h = 4 * (x8 & 3) + (j & 3), tile = 8 * (x8 >> 2) + (j >> 2);
  const u32 r0_ = 2 * (h & 3) + (tile & 7);
  const int rot = (int)(r0_ < r0_ - 7u ? r0_ : r0_ - 7u);
  char* ws = p.ws;
  LDECL
  STAMP_DECL
  STAMP(0);
  __builtin_amdgcn_s_setprio(3);
  const u32 lb = ldsaddr(lds);
  const int tok0 = tile * 128 + w * 16;
  u32 xvo[2];
#pragma unroll
  for (int i = 0; i < 2; ++i) { int row = 8 * i + (lane >> 3); xvo[i] = (tok0 + row) * (D * 2) + (((lane & 7) ^ ((row >> 1) & 7)) * 16); }
  u32 wvo;
  { int row = 16 * (w & 3) + (lane >> 2); int cs = (lane & 3) ^ (2 * ((row >> 3) & 1)); wvo = row * WROW + h * (DH * 2) + (cs < 3 ? cs * 16 : 40); }
  {
    const u32 soW0 = rot * WCH + (1 + (w >> 2)) * (D * 2), soX0 = rot * 128, dW0 = lb + A_W + w * 1024, dX0 = lb + A_X + w * 2048;
    asm volatile("" ::"v"(xvo[0]), "v"(xvo[1]), "v"(wvo), "s"(soW0), "s"(soX0), "s"(dW0), "s"(dX0));
    __builtin_amdgcn_sched_barrier(0);
  }
  const u32x4 rX = mkrs4(p.in[0], S * D * 2), rW = mkrs4(p.in[1], D * 3 * D * 2);
  const rsrc_t rWS = mkrs(ws, WS_END);
  const u32x4 rWS4 = mkrs4(ws, WS_END);
  const u32 gen = sload(ws + WS_GEN);
  auto cidx = [&](int i) { int c = rot + i; return c >= 7 ? c - 7 : c; };
  auto issueX = [&](int i) {
    const u32 so = cidx(i) * 128, dst = lb + A_X + (i & 1) * 16384 + w * 2048;
    adma16(rX, dst, xvo[0], so); adma16(rX, dst + 1024, xvo[1], so);
  };
  auto issueW = [&](int v) {
    const u32 dst = lb + A_W + (v % 5) * 8192 + w * 1024;
    if (v < 7) adma16(rW, dst, wvo, cidx(v) * WCH + (1 + (w >> 2)) * (D * 2));
    else adma16(rW, dst, wvo, cidx(2 * (v - 7) + (w >> 2)) * WCH);
  };
  auto issueSmall = [&]() {
    if (w < 2) adma16(mkrs4(p.in[2], H * DH * DH * 2), lb + A_WPHI + w * 1024, lane * 16, h * (DH * DH * 2) + w * 1024);
    else if (w == 2) { int c = rot + (lane >> 3); c = c >= 7 ? c - 7 : c; adma16(mkrs4(p.in[13], D * 2), lb + A_GW, lane < 56 ? c * 128 + (lane & 7) * 16 : 4096, 0); }
  };
  LST(12);
  issueW(0);
  LST(13);
  issueX(0);
  LST(14);
  issueSmall();
  LST(24);
  __builtin_amdgcn_s_setprio(2);
  issueW(1);
  LST(25);
  issueX(1);
  LST(15);
  __builtin_amdgcn_s_setprio(1);
  issueW(2);
  const bool g0 = w < 4;
  if (g0) issueW(3);
  LST2(6);
  __builtin_amdgcn_s_setprio(APRIO);
  __builtin_amdgcn_sched_barrier(0);
  STAMP(1);
  PRB(1);
  f32x4 ak[2] = {{0, 0, 0, 0}, {0, 0, 0, 0}}, av[2] = {{0, 0, 0, 0}, {0, 0, 0, 0}};
  u32 xoff[2];
#pragma unroll
  for (int ks = 0; ks < 2; ++ks) xoff[ks] = w * 2048 + l15 * 128 + (((4 * ks + g) ^ ((l15 >> 1) & 7)) * 16);
  u32 woff[2];
#pragma unroll
  for (int ct = 0; ct < 2; ++ct) woff[ct] = (8 * g + q4) * 64 + ((ct ^ (g & 1)) * 32) + pp * 8;
  u32x4 xf[14];
  const bool isg = w == (h >> 1) && ((h ^ tile) & 1) == 0;
  u32x4 wf0[2][2], wf1[2][2];
  auto readX = [&](int i) {
    const char* xs = lds + A_X + (i & 1) * 16384;
    xf[2 * i] = *(const u32x4*)(xs + xoff[0]); xf[2 * i + 1] = *(const u32x4*)(xs + xoff[1]);
  };
  auto readW = [&](int i, int ks, u32x4 (&wf)[2][2]) {
    const char* wsb = lds + A_W + (i % 5) * 8192;
#pragma unroll
    for (int r = 0; r < 2; ++r)
#pragma unroll
      for (int ct = 0; ct < 2; ++ct) {
        u32x2 a = trread(wsb, woff[ct] + r * 4096 + ks * 2048);
        u32x2 b = trread(wsb, woff[ct] + r * 4096 + ks * 2048 + 256);
        wf[r][ct] = (u32x4){a[0], a[1], b[0], b[1]};
      }
  };
  auto mm = [&](const u32x4& x, const u32x4 (&wf)[2][2]) {
#pragma unroll
    for (int ct = 0; ct < 2; ++ct) { ak[ct] = mfma16(wf[0][ct], x, ak[ct]); av[ct] = mfma16(x, wf[1][ct], av[ct]); }
  };
  if (tid < 2) *(volatile LAS u32*)(LAS char*)(lds + A_CNT + tid * 4) = 0;
  if (g0) waitv<4>(); else waitv<3>();
  LST2(7);
  PRB(20);
  bar_l();
  readX(0); readW(0, 0, wf0);
  __builtin_amdgcn_sched_barrier(0);
  readW(0, 1, wf1);
  __builtin_amdgcn_sched_barrier(0);
  WAIT_L(8);
  if (!g0) { issueX(2); issueW(3); }
#pragma unroll
  for (int i = 0; i < 7; ++i) {
    __builtin_amdgcn_sched_barrier(0);
    if (g0) {
      if (i <= 4) issueX(i + 2);
      if (i <= 2) issueW(i + 4);
      if (i == 4) { issueW(7); issueW(8); }
      if (i == 5) { issueW(9); adma16(rW, lb + A_Q6 + w * 1024, wvo, cidx(6) * WCH); }
    } else if (i == 5) issueW(9);
    __builtin_amdgcn_sched_barrier(0);
    mm(xf[2 * i], wf0);
    __builtin_amdgcn_sched_barrier(0);
    if (i < 6) {
      if (g0) { if (i == 0) waitv<5>(); else if (i == 3) waitv<3>(); else waitv<4>(); }
      else { if (i < 4) waitv<4>(); else waitv<3>(); }
      LST(2 * i);
      readX(i + 1); readW(i + 1, 0, wf0);
    }
    __builtin_amdgcn_sched_barrier(0);
    mm(xf[2 * i + 1], wf1);
    __builtin_amdgcn_sched_barrier(0);
    if (i == 1 && w == 7) {
      u32x4 wph[2];
#pragma unroll
      for (int ft = 0; ft < 2; ++ft) {
        int f = 7 * q4 + 4 * ft + pp; f = f > 27 ? 27 : f;
        const char* wp = lds + A_WPHI + f * 2;
        const int d0 = 4 * g, d1 = g < 2 ? 16 + 4 * g : 24;
        u32 e[8];
#pragma unroll
        for (int jj = 0; jj < 4; ++jj) { e[jj] = *(const u16*)(wp + (d0 + jj) * 56); e[4 + jj] = *(const u16*)(wp + (d1 + jj) * 56); }
        wph[ft] = (u32x4){e[0] | (e[1] << 16), e[2] | (e[3] << 16), g == 2 ? 0u : (e[4] | (e[5] << 16)), g == 2 ? 0u : (e[6] | (e[7] << 16))};
      }
      CFENCE();
      *(u32x4*)(lds + A_WPHI + lane * 16) = wph[0]; *(u32x4*)(lds + A_WPHI + 1024 + lane * 16) = wph[1];
      WAIT_L(0);
    }
    if (i < 6) {
      readW(i + 1, 1, wf1);
      __builtin_amdgcn_sched_barrier(0);
      WAIT_L(8);
      if (i < 5) {
        if (!g0) {
          if (i <= 3) issueX(i + 3);
          if (i <= 2) issueW(i + 4);
          if (i == 4) { issueW(7); issueW(8); }
          if (i <= 2) waitv<6>(); else if (i == 3) waitv<5>(); else waitv<4>();
        } else if (i == 0) waitv<4>();
        LST(2 * i + 1);
        BAR();
        CFENCE();
        LST2(i);
      }
    }
  }
  __builtin_amdgcn_s_setprio(APRIO2);
  STAMP(2);
  PRB(2);
  const u32 tag = gen + 1u, t16 = tag & 0xffffu;
  char* phk = lds + A_X; char* vt = lds + A_VT;
  {
    const int tl = w * 16 + l15;
    u32x4 kb = {pk2(ak[0][0], ak[0][1]), pk2(ak[0][2], ak[0][3]), pk2(ak[1][0], ak[1][1]), pk2(ak[1][2], ak[1][3])};
    f32x4 z4 = {0, 0, 0, 0}; f32x4 pk[2];
    pk[0] = mfma16(*(const u32x4*)(lds + A_WPHI + lane * 16), kb, z4); pk[1] = mfma16(*(const u32x4*)(lds + A_WPHI + 1024 + lane * 16), kb, z4);
    u32x4 pos, neg; dual_softmax(pk, pos, neg);
    const int sw = ((tl >> 1) & 1) | (((tl >> 3) & 1) << 1);
    char* rowp = phk + tl * 128 + g * 8;
    *(u32x2*)(rowp + ((0 ^ sw) * 32)) = (u32x2){pos[0], pos[1]};
    *(u32x2*)(rowp + ((1 ^ sw) * 32)) = (u32x2){pos[2], pos[3]};
    *(u32x2*)(rowp + ((2 ^ sw) * 32)) = (u32x2){neg[0], neg[1]};
    *(u32x2*)(rowp + ((3 ^ sw) * 32)) = (u32x2){neg[2], neg[3]};
  }
  KD(9);
  {
#pragma unroll
    for (int ct = 0; ct < 2; ++ct) {
      int d = ct == 0 ? l15 : (l15 < 8 ? 16 + l15 : 12 + l15);
      int G = (d * 37) >> 8, e = d - 7 * G;
      int rho = e < 4 ? 4 * G + e : 12 + 4 * G + e;
      f32x4 v = av[ct];
      if (ct == 1 && (l15 >> 2) == 2) { rho = pp == 0 ? 27 : (pp == 1 ? 19 : (pp == 2 ? 23 : 31)); if (pp == 0) v = (f32x4){1.f, 1.f, 1.f, 1.f}; }
      *(u32x2*)(vt + rho * 256 + (((2 * w + (g >> 1)) ^ (rho & 15)) * 16) + (g & 1) * 8) = (u32x2){pk2(v[0], v[1]), pk2(v[2], v[3])};
    }
  }
  const bool fin = j < 28;
  const int tt = 4 * x8 + j / 7, cg = j % 7, c0 = cg * 64;
  const int rt = w >> 1, kh = w & 1;
  const int ftok = tt * 64 + rt * 16 + l15;
  const u32 tw16 = t16 << 16;
  auto issueWO = [&](int u0, int m0, int n, int ms) {
    const u32x4 rWO = mkrs4(p.in[3], D * D * 2);
    const int jr = lane >> 3;
    const u32 vo = (jr < 7 ? jr : 6) * (D * 2) + cg * 128 + (((lane & 7) ^ (2 * (((jr >> 1) & 1) | ((w & 1) << 1)))) * 16);
#pragma unroll
    for (int m = 0; m < 16; ++m) if (m < n) adma16(rWO, lb + (u0 + 4 * (m0 + ms * m)) * 1024, vo, (u0 + 4 * (m0 + ms * m)) * (7 * D * 2));
  };
  KD(10);
  waitv<0>();
  KD(12);
  bar_l();
  KD(3);
  int fin0 = j; asm volatile("" : "+s"(fin0));
  const int rw = (h >> 1) == 7 ? 6 : 7;
  if (w >= 4 && w != rw && fin0 < 28) issueWO(w & 3, 7, 5, 1);
  if (w < 4) {
    const int sg = w >> 1, dt = w & 1;
    f32x4 t0 = {0, 0, 0, 0}, t1 = t0;
#pragma unroll
    for (int ks = 0; ks < 4; ++ks) {
      u32x4 af[2];
#pragma unroll
      for (int ft = 0; ft < 2; ++ft) {
        u32x2 a[2];
#pragma unroll
        for (int t = 0; t < 2; ++t) {
          int row = 32 * ks + 8 * g + 4 * t + q4;
          int sw = ((row >> 1) & 1) | (((row >> 3) & 1) << 1);
          a[t] = trread(phk, row * 128 + (((2 * sg + ft) ^ sw) * 32) + pp * 8);
        }
        af[ft] = (u32x4){a[0][0], a[0][1], a[1][0], a[1][1]};
      }
      u32x4 bf = *(const u32x4*)(vt + (16 * dt + l15) * 256 + (((4 * ks + g) ^ l15) * 16));
      t0 = mfma16(af[0], bf, t0); t1 = mfma16(af[1], bf, t1);
    }
    KD(11);
    __builtin_amdgcn_sched_barrier(0);
    if (lane == 0) __hip_atomic_fetch_add((LAS u32*)(LAS char*)(lds + A_CNT + 4), 1u, __ATOMIC_RELAXED, __HIP_MEMORY_SCOPE_WORKGROUP);
    u32x4 un = {pk2(t0[0], t0[1]), pk2(t0[2], t0[3]), pk2(t1[0], t1[1]), (pk2(t1[2], t1[2]) & 0xffffu) | (t16 << 16)};
    st16c(un, rWS, (((h * 16 + tile) * 4 + w) * 64 + lane) * 16, WS_KVP);
    if (w == (rw & 3) && fin0 < 28) issueWO(w, 7, 5, 1);
  }
  STAMP(5);
  PRB(5);
  if (w == rw) __builtin_amdgcn_s_setprio(3); else
  __builtin_amdgcn_s_setprio(APRIO3);
  u32x4 pqp, pqn;
  f32x4 pq[2];
  {
    f32x4 aq[2] = {{0, 0, 0, 0}, {0, 0, 0, 0}};
#pragma unroll
    for (int i = 0; i < 7; ++i) {
      const char* qb = lds + (i == 6 ? A_Q6 : A_W + ((7 + (i >> 1)) % 5) * 8192 + (i & 1) * 4096);
#pragma unroll
      for (int ks = 0; ks < 2; ++ks)
#pragma unroll
        for (int ct = 0; ct < 2; ++ct) {
          u32x2 a = trread(qb, woff[ct] + ks * 2048);
          u32x2 b = trread(qb, woff[ct] + ks * 2048 + 256);
          aq[ct] = mfma16((u32x4){a[0], a[1], b[0], b[1]}, xf[2 * i + ks], aq[ct]);
        }
    }
    if (isg) {
      f32x4 ga = {0, 0, 0, 0};
#pragma unroll
      for (int i = 0; i < 14; ++i) ga = mfma16(*(const u32x4*)(lds + A_GW + i * 64 + g * 16), xf[i], ga);
      const float gate = rbf(__builtin_amdgcn_rcpf(1.f + __expf(-rbf(ga[0]))));
      if (lane < 16) st4c((__float_as_uint(gate) >> 16) | (t16 << 16), rWS, (tok0 + lane) * 4, WS_GATE);
    }
    const u32x4 wq0 = *(const u32x4*)(lds + A_WPHI + lane * 16), wq1 = *(const u32x4*)(lds + A_WPHI + 1024 + lane * 16);
    u32x4 qb = {pk2(aq[0][0], aq[0][1]), pk2(aq[0][2], aq[0][3]), pk2(aq[1][0], aq[1][1]), pk2(aq[1][2], aq[1][3])};
    f32x4 z4 = {0, 0, 0, 0};
    pq[0] = mfma16(wq0, qb, z4); pq[1] = mfma16(wq1, qb, z4);
    __builtin_amdgcn_sched_barrier(0);
    if (lane == 0) __hip_atomic_fetch_add((LAS u32*)(LAS char*)(lds + A_CNT), 1u, __ATOMIC_RELAXED, __HIP_MEMORY_SCOPE_WORKGROUP);
  }
  if ((w >= 4 && w != rw) || w == (rw & 3)) {
    int fin2 = j; asm volatile("" : "+s"(fin2));
    if (fin2 < 28) {
#pragma unroll 1
      for (int it = 0; it < MAXSPIN; ++it) {
        if (*(volatile LAS u32*)(LAS char*)(lds + A_CNT + 4) >= 4u) break;
        __builtin_amdgcn_s_sleep(1);
      }
      CFENCE();
      issueWO(w & 3, 0, 6, 1);
    }
  }
  if (w == rw) {
    __builtin_amdgcn_s_setprio(3);
    const u32 ro = ((h * 16 + g) * 4 + (tile >> 2)) * 1024 + (16 * (tile & 3) + l15) * 16;
    u32x4 rsel;
#pragma unroll
    for (int q = 0; q < 4; ++q) rsel[q] = l15 == 2 * q ? 0x00003f80u : (l15 == 2 * q + 1 ? 0x3f800000u : 0u);
    asm volatile("" : "+v"(rsel));
    u32x4 pv[4];
#pragma unroll 1
    for (int it = 0; it < MAXSPIN; ++it) {
#pragma unroll
      for (int i = 0; i < 4; ++i) pv[i] = ld16c(rWS, ro, WS_KVP + i * 16384);
      const u32 m = (pv[0][3] ^ tw16) | (pv[1][3] ^ tw16) | (pv[2][3] ^ tw16) | (pv[3][3] ^ tw16);
      if (__all(m < 0x10000u)) break;
      __builtin_amdgcn_s_sleep(1);
    }
    f32x4 rs_ = {0, 0, 0, 0};
#pragma unroll
    for (int t = 0; t < 4; ++t) { u32x4 d = pv[t]; d[3] &= 0xffffu; rs_ = mfma16(rsel, d, rs_); }
    auto e0 = __builtin_amdgcn_permlane16_swap(pk2(rs_[0], rs_[1]), pk2(rs_[0], rs_[1]), false, false);
    auto e1 = __builtin_amdgcn_permlane16_swap(pk2(rs_[2], rs_[3]), pk2(rs_[2], rs_[3]), false, false);
    if (lane < 16) st16c((u32x4){e0[0], e1[0], e0[1], (e1[1] & 0xffffu) | tw16}, rWS, h * 4096 + tile * 256 + lane * 16, WS_KVR);
    __builtin_amdgcn_s_setprio(APRIO3);
  }
  __builtin_amdgcn_sched_barrier(0);
  dual_softmax(pq, pqp, pqn);
  __builtin_amdgcn_sched_barrier(0);
  STAMP(6);
  PRB(6);
  __builtin_amdgcn_s_setprio(APRIO4);
  {
#pragma unroll 1
    for (int it = 0; it < MAXSPIN; ++it) {
      if (*(volatile LAS u32*)(LAS char*)(lds + A_CNT) >= 8u) break;
      __builtin_amdgcn_s_sleep(1);
    }
    CFENCE();
    int fin1 = j; asm volatile("" : "+s"(fin1));
    if (w >= 4) {
      if (fin1 < 28) { issueWO(w & 3, 6, 1, 1); issueWO(w & 3, 12, 4, 1); }
    } else {
      u32x4 kr;
#pragma unroll 1
      for (int it = 0; it < MAXSPIN; ++it) {
        kr = ld16c(rWS, h * 4096 + w * 1024 + lane * 16, WS_KVR);
        if (__all((kr[3] ^ tw16) < 0x10000u)) break;
        __builtin_amdgcn_s_sleep(1);
      }
      kr[3] &= 0xffffu;
      *(u32x4*)(lds + A_KVB + w * 1024 + lane * 16) = kr;
    }
  }
  bar_l();
  STAMP(7);
  PRB(7);
  {
    f32x4 o[2];
#pragma unroll
    for (int dt = 0; dt < 2; ++dt) {
      const u32x4 kp = *(const u32x4*)(lds + A_KVB + dt * 1024 + lane * 16), kn = *(const u32x4*)(lds + A_KVB + (2 + dt) * 1024 + lane * 16);
      o[dt] = mfma16(kp, pqp, (f32x4){0, 0, 0, 0});
      o[dt] = mfma16(kn, pqn, o[dt]);
    }
    u32 du = __float_as_uint(o[1][3]);
    auto r1 = __builtin_amdgcn_permlane32_swap(du, du, false, false);
    u32 d1 = r1[1];
    auto r2 = __builtin_amdgcn_permlane16_swap(d1, d1, false, false);
    float den = __uint_as_float(r2[0]);
    den = rbf(rbf(den) + 1e-6f);
    const float rd = __builtin_amdgcn_rcpf(den);
    const u32 n0 = pk2(o[0][0], o[0][1]), n1 = pk2(o[0][2], o[0][3]), n2 = pk2(o[1][0], o[1][1]), n3 = pk2(o[1][2], o[1][2]);
    u32x4 of = {pk2(bflo(n0) * rd, bfhi(n0) * rd), pk2(bflo(n1) * rd, bfhi(n1) * rd), pk2(bflo(n2) * rd, bfhi(n2) * rd), (pk2(bflo(n3) * rd, 0.f) & 0xffffu) | (t16 << 16)};
    const int rt16 = tile * 8 + w;
    LFLUSH(ws);
    st16c(of, rWS, ((rt16 * 16 + h) * 64 + lane) * 16, WS_OF);
  }
  STAMP(8);
  PRB(8);
  if (fin) {
    const int rt16 = tt * 4 + rt;
    u32 ooff = (ftok * D + c0 + 32 * kh + 8 * g) * 2;
    waitv<1>();
    BAR();
    CFENCE();
    u32 wa[4];
    { const int s = 2 * (((q4 >> 1) & 1) | ((g & 1) << 1));
#pragma unroll
      for (int ct = 0; ct < 4; ++ct) wa[ct] = kh * 32768 + (8 * g + q4) * 128 + (((2 * ct) ^ s) * 16) + pp * 8; }
    u32x4 wf[NPRE][4];
    auto readWO = [&](int hh, u32x4 (&f)[4]) {
#pragma unroll
      for (int ct = 0; ct < 4; ++ct) {
        u32x2 a = trread(lds, wa[ct] + hh * 4096);
        u32x2 b = trread(lds, wa[ct] + hh * 4096 + 512);
        f[ct] = (u32x4){a[0], a[1], b[0], b[1]};
      }
    };
#pragma unroll
    for (int hh = 0; hh < NPRE; ++hh) readWO(hh, wf[hh]);
    bar_l();
    u32x4 of[8];
    u32 gv;
    bool yok, yok2 = false;
    const u32 ovo = ((rt16 * 16 + 8 * kh) * 64 + lane) * 16;
    const u32 yfo = WS_YF + ((c0 >> 1) + 4 * w + (lane & 3)) * 1024 + x8 * 128 + (YW ? ((lane >> 2) & 1) * 64 : 0);
    const u32 tw = t16 << 16;
    const u32 yvo = (c0 + 8 * w + (lane >> 3)) * YSTR + tt * 128 + (((lane & 7) ^ (2 * ((2 * w + (lane >> 5)) & 3))) * 16);
    u32 yd = 0;
    {
      const u32 po = lane < 8 ? WS_OF + ((rt16 * 16 + 8 * kh + lane) * 64 + 63) * 16 + 12 : (lane < 16 ? WS_GATE + (rt16 * 16 + 15) * 4 : yfo);
      const G8 go = {rWS4, ovo, po, WS_OF, 1024u, 0xffffffffu, WS_OF, 0x1ffu, t16, yvo, WS_YT, lb + w * 1024};
      u32 dn = 0, pl2 = ld4c(rWS, po, 0);
      g8_first(go, of, pl2, dn, yd);
      g8_rest(go, of, pl2, dn, yd);
      yok = __all(lane < 16 || (pl2 >> 16) == t16);
      gv = ld4c(rWS, ftok * 4, WS_GATE);
      u32 yf2 = ld4c(rWS, yfo, 0);
      wait8n<2>(of);
#pragma unroll 1
      for (int it = 0; it < MAXSPIN; ++it) {
        u32 m = gv ^ tw;
#pragma unroll
        for (int t = 0; t < 8; ++t) m |= of[t][3] ^ tw;
        if (__all(m < 0x10000u)) break;
        __builtin_amdgcn_s_sleep(2);
        gv = ld4c(rWS, ftok * 4, WS_GATE);
        g8_reload(go, of);
      }
      yok2 = __all((yf2 >> 16) == t16);
    }
    FSTAMP(12);
    yok = yok || yok2;
    if (yok && !yd) adma16(rWS4, lb + w * 1024, yvo, WS_YT);
    __builtin_amdgcn_sched_barrier(0);
    FSTAMP(9);
    PRB(9);
    f32x4 acc[4];
#pragma unroll
    for (int ct = 0; ct < 4; ++ct) acc[ct] = (f32x4){0, 0, 0, 0};
#pragma unroll
    for (int hh = 0; hh < 8; ++hh) {
      u32x4 ob = of[hh]; ob[3] &= 0xffffu;
#pragma unroll
      for (int ct = 0; ct < 4; ++ct) acc[ct] = mfma16(wf[hh % NPRE][ct], ob, acc[ct]);
      if (hh + NPRE < 8) readWO(hh + NPRE, wf[hh % NPRE]);
      __builtin_amdgcn_sched_barrier(0);
    }
    FSTAMP(10);
    const f32x4 c0_ = acc[0], c1_ = acc[1], c2_ = acc[2], c3_ = acc[3];
    {
      f32x4* rw = (f32x4*)(lds + A_RED + w * 2048) + lane;
      rw[0] = kh ? c0_ : c2_; rw[64] = kh ? c1_ : c3_;
    }
    const f32x4 keep0 = kh ? c2_ : c0_, keep1 = kh ? c3_ : c1_;
    if (!yok && !yd) {
#pragma unroll 1
      for (int it = 0; it < MAXSPIN; ++it) {
        const u32 v = ld4c(rWS, yfo, 0);
        if (__all((v >> 16) == t16)) break;
        __builtin_amdgcn_s_sleep(2);
      }
      adma16(rWS4, lb + w * 1024, yvo, WS_YT);
    }
    u32 rro = A_RED + (w ^ 1) * 2048 + lane * 16;
    u32 ybo = (32 * kh + 4 * g) * 128 + (((2 * rt + (l15 >> 3)) ^ (2 * g)) * 16) + (l15 & 7) * 2;
    float gg = bflo(gv), og = rbf(1.f - gg);
    asm volatile("" : "+v"(rro), "+v"(ybo), "+v"(gg), "+v"(og), "+v"(ooff));
    waitv<0>();
    bar_l();
    DBGCNT(st_[3] = (u32)__builtin_amdgcn_s_memrealtime();)
    f32x4 ra[2]; u32 yb[2][4];
#pragma unroll
    for (int m = 0; m < 2; ++m) ra[m] = *(const f32x4*)(lds + rro + m * 1024);
#pragma unroll
    for (int m = 0; m < 2; ++m)
#pragma unroll
      for (int i = 0; i < 4; ++i) yb[m][i] = *(const u16*)(lds + ybo + (16 * m + i) * 128);
    __builtin_amdgcn_sched_barrier(0);
    u32 res[2][2];
#pragma unroll
    for (int m = 0; m < 2; ++m) {
      const f32x4 a = ra[m] + (m ? keep1 : keep0);
#pragma unroll
      for (int q = 0; q < 2; ++q) {
        const u32 a2 = pk2(a[2 * q], a[2 * q + 1]);
        const u32 p2 = pk2(smul(gg, bflo(a2)), smul(gg, bfhi(a2)));
        const u32 y2 = pk2(smul(og, bflo(yb[m][2 * q])), smul(og, bflo(yb[m][2 * q + 1])));
        res[m][q] = pk2(bflo(p2) + bflo(y2), bfhi(p2) + bfhi(y2));
      }
    }
    u32x4 ov;
    {
      auto s0 = __builtin_amdgcn_permlane32_swap(res[0][0], res[1][0], false, false);
      auto s1 = __builtin_amdgcn_permlane32_swap(res[0][1], res[1][1], false, false);
      auto t0 = __builtin_amdgcn_permlane16_swap(s0[0], s0[1], false, false);
      auto t1 = __builtin_amdgcn_permlane16_swap(s1[0], s1[1], false, false);
      ov = (u32x4){t0[0], t1[0], t0[1], t1[1]};
    }
    __builtin_amdgcn_raw_buffer_store_b128(ov, mkrs(p.out, S * D * 2), ooff, 0, OUTAUX);
    FSTAMP(11);
  }
  else if (x8 == 0 && j == 31) {
    if (tid < 256) {
      const u32 off = tid < 224 ? WS_YF + tid * 1024 : WS_OF + ((tid - 224) & 15) * 1024 + 12;
#pragma unroll 1
      for (int it = 0; it < MAXSPIN; ++it) {
        const u32 v = ld4c(rWS, off, 0);
        if (__all((v >> 16) == t16)) break;
        __builtin_amdgcn_s_sleep(16);
      }
    }
    CFENCE();
    BAR();
    if (tid == 0) __hip_atomic_store((u32*)(ws + WS_GEN), tag, __ATOMIC_RELAXED, __HIP_MEMORY_SCOPE_AGENT);
  }
  DBGCNT(st_[4] = tent;)
  STAMP_FLUSH(ws);
}

DI f32x2 cmul(f32x2 a, f32x2 b) {
  float t0, t1, re, im;
  asm("v_mul_f32 %0, %1, %2" : "=v"(t0) : "v"(a.y), "v"(b.y));
  asm("v_mul_f32 %0, %1, %2" : "=v"(t1) : "v"(a.y), "v"(b.x));
  asm("v_fma_f32 %0, %1, %2, -%3" : "=v"(re) : "v"(a.x), "v"(b.x), "v"(t0));
  asm("v_fma_f32 %0, %1, %2, %3" : "=v"(im) : "v"(a.x), "v"(b.y), "v"(t1));
  return (f32x2){re, im};
}
DI f32x2 cexp_pow(float mr, float pr, float a) {
  float m = __builtin_amdgcn_exp2f(mr * a);
  float t = __builtin_amdgcn_fractf(pr * a);
  return (f32x2){m * __builtin_amdgcn_cosf(t), m * __builtin_amdgcn_sinf(t)};
}
constexpr int KG_ROW = 272, KG_SLAB = 16 * KG_ROW;
constexpr int B_UC = 34816, UCH = 16656, B_KR = 68128, B_KTAB = 77344, B_DUM = 80416, B_CNT = 80928, B_TIMG = B_KR, B_RED = 0;
constexpr int UC0 = 8, UC1 = 4176, UC2 = 8336, UC3 = 12496;

struct BPre { u32 are, aim, bre, bim, cre, cim, ldt, dskip, gen, pf[9]; };
DI void preB(const Params& p, int x8, int jb, char* lds, BPre& q) {
  const int tid = threadIdx.x, lane = tid & 63, w = __builtin_amdgcn_readfirstlane(tid >> 6);
  const int d = 2 * (x8 * 28 + jb) + (w >> 2);
  const u32 mob = (u32)(d * NM + (tid & 255)) * 2u;
  auto ldp = [&](int k, u32 byte) { return (u32)*(const u16*)((const char*)p.in[k] + byte); };
  q.ldt = ldp(11, (u32)d * 2u);
  q.are = ldp(5, mob); q.aim = ldp(6, mob); q.bre = ldp(7, mob); q.bim = ldp(8, mob); q.cre = ldp(9, mob); q.cim = ldp(10, mob);
  q.dskip = ldp(12, (u32)d * 2u);
  __builtin_amdgcn_sched_barrier(0);
  const int sl = (x8 & 3) * 28 + jb, tt = 16 * (x8 >> 2) + sl / 7, cgp = sl % 7;
  adma16(mkrs4(p.in[0], S * D * 2), ldsaddr(lds) + B_UC + w * 1024, (tt * 64 + 8 * w + (lane >> 3)) * (D * 2) + (((lane & 7) ^ w) * 16), cgp * 128);
  __builtin_amdgcn_sched_barrier(0);
}
DI void roleB(const Params& p, int x8, int jb, char* lds, u32 tent, const BPre& q) {
  const int tid = threadIdx.x, lane = tid & 63, w = __builtin_amdgcn_readfirstlane(tid >> 6);
  const int g = lane >> 4, l15 = lane & 15;
  char* ws = p.ws;
  STAMP_DECL
  STAMP(0);
  const rsrc_t rWS = mkrs(ws, WS_END);
  const int ch0 = 2 * (x8 * 28 + jb);
  const int sl = (x8 & 3) * 28 + jb, tt = 16 * (x8 >> 2) + sl / 7, cgp = sl % 7;
  u32 are = q.are, aim = q.aim, bre = q.bre, bim = q.bim, cre = q.cre, cim = q.cim, ldt = q.ldt, dskip = q.dskip;
  u32 gen = sload(ws + WS_GEN);
  asm volatile("" : "+v"(are), "+v"(aim), "+v"(bre), "+v"(bim), "+v"(cre), "+v"(cim), "+v"(ldt), "+v"(dskip));
  if (tid == 0) *(volatile LAS u32*)(LAS char*)(lds + B_CNT) = 0;
  __builtin_amdgcn_s_setprio(BPRIO);
  STAMP(1);
  auto xpose = [&]() {
    waitv<0>();
    WAIT_L(0);
    BAR();
    CFENCE();
    {
      const int chl = lane >> 3, oc = lane & 7;
      const char* img = lds + B_UC + 8 * oc * 128 + ((w ^ oc) * 16) + chl * 2;
      u32 o[8];
#pragma unroll
      for (int jj = 0; jj < 8; ++jj) o[jj] = *(const u16*)(img + jj * 128);
      st16c((u32x4){o[0] | (o[1] << 16), o[2] | (o[3] << 16), o[4] | (o[5] << 16), o[6] | (o[7] << 16)}, rWS, (cgp * 64 + 8 * w + chl) * XSTR + tt * 128 + oc * 16, WS_XT);
    }
  };
  u32 tag, xfl0 = 0;
  char* slab = lds + w * KG_SLAB;
  const int cch = tid >> 8, cq = tid & 255;
  const u32 uvo = (ch0 + cch) * XSTR + cq * 16;
  f32x4 acc[3];
  {
    const float dt = rbf(__expf(bflo(ldt)));
    const float ar = dt * bflo(are), ai = dt * bflo(aim), br = dt * bflo(bre), bi = dt * bflo(bim), cr = bflo(cre), ci = bflo(cim);
    const float mr = ar * 1.4426950408889634f, pr = ai * 0.15915494309189535f;
    f32x2 W = {cr * br - ci * bi, -(cr * bi + ci * br)};
    f32x2 z1c = cexp_pow(mr, -pr, 1.f);
    f32x2 z8 = cexp_pow(mr, pr, 8.f);
    f32x2 a128 = cexp_pow(mr, -pr, 128.f);
    asm volatile("s_mov_b32 m0, %0" ::"s"(ldsaddr(slab)) : "memory");
#define WROWT(r, v) do { const f32x2 v_ = (v); asm volatile("ds_write_addtid_b32 %0 offset:%1" ::"v"(pk2(v_.x, v_.y)), "i"((r) * KG_ROW) : "memory"); } while (0)
    const char* rd = slab + l15 * KG_ROW + g * 16;
    u32x4 yf[4];
    {
      f32x2 y[16];
      y[1] = z8;
      f32x2 y1c = z8; asm volatile("" : "+v"(y1c));
      y[2] = cmul(y[1], y1c); y[3] = cmul(y[2], y[1]); y[4] = cmul(y[3], y[1]);
      y[5] = cmul(y[4], y[1]); y[6] = cmul(y[4], y[2]); y[7] = cmul(y[4], y[3]); y[8] = cmul(y[5], y[3]);
#pragma unroll
      for (int c = 9; c < 16; ++c) y[c] = cmul(y[8], y[c - 8]);
      WROWT(0, ((f32x2){1.f, 0.f}));
#pragma unroll
      for (int c = 1; c < 16; ++c) WROWT(c, y[c]);
    }
#pragma unroll
    for (int ks = 0; ks < 4; ++ks) yf[ks] = *(const u32x4*)(rd + ks * 64);
    CFENCE();
    f32x2 R0[8], t1[8], a2;
    {
      f32x2 zc = z1c; asm volatile("" : "+v"(zc));
      const f32x2 z2 = cmul(z1c, zc);
      R0[0] = W; R0[1] = cmul(W, z1c);
#pragma unroll
      for (int b = 2; b < 8; ++b) R0[b] = cmul(R0[b - 2], z2);
      f32x2 a1c = a128; asm volatile("" : "+v"(a1c));
      a2 = cmul(a128, a1c);
#pragma unroll
      for (int b = 0; b < 8; ++b) t1[b] = cmul(R0[b], a128);
#pragma unroll
      for (int b = 0; b < 8; ++b) { WROWT(b, R0[b]); WROWT(8 + b, t1[b]); }
    }
    f32x2 t6, t7;
    acc[0] = (f32x4){0, 0, 0, 0};
#pragma unroll
    for (int ks = 0; ks < 4; ++ks) { u32x4 xf = *(const u32x4*)(rd + ks * 64); acc[0] = mfma16(xf, yf[ks], acc[0]); }
    CFENCE();
    STAMP(7);
    xpose();
    {
      f32x2 t[16];
#pragma unroll
      for (int b = 0; b < 4; ++b) { t[b] = cmul(R0[2 * b], a2); t[4 + b] = cmul(t1[2 * b], a2); }
#pragma unroll
      for (int b = 0; b < 2; ++b) { t[8 + b] = cmul(t[2 * b], a2); t[10 + b] = cmul(t[4 + 2 * b], a2); }
#pragma unroll
      for (int b = 0; b < 4; ++b) t[12 + b] = cmul(t[8 + b], a2);
      t6 = t[12]; t7 = t[14];
#pragma unroll
      for (int r = 0; r < 16; ++r) WROWT(r, t[r]);
    }
    acc[1] = (f32x4){0, 0, 0, 0};
#pragma unroll
    for (int ks = 0; ks < 4; ++ks) { u32x4 xf = *(const u32x4*)(rd + ks * 64); acc[1] = mfma16(xf, yf[ks], acc[1]); }
    CFENCE();
    STAMP(9);
    asm volatile("" : "+s"(gen));
    tag = gen + 1u;
    waitv<0>();
    if (lds_inc(ldsaddr(lds) + B_CNT) == 7u && lane < 8) st16c((u32x4){tag, tag, tag, tag}, rWS, (cgp * 32 + tt) * 128 + lane * 16, WS_XF);
    __builtin_amdgcn_sched_barrier(0);
    {
      f32x2 t[9];
      t[0] = cmul(t6, a2); t[1] = cmul(t7, a2);
#pragma unroll
      for (int hh = 2; hh < 9; ++hh) t[hh] = cmul(t[hh - 2], a2);
#pragma unroll
      for (int hh = 0; hh < 9; ++hh) WROWT(hh, t[hh]);
    }
    acc[2] = (f32x4){0, 0, 0, 0};
#pragma unroll
    for (int ks = 0; ks < 4; ++ks) { u32x4 xf = *(const u32x4*)(rd + ks * 64); acc[2] = mfma16(xf, yf[ks], acc[2]); }
    CFENCE();
    float* kt = (float*)slab;
    *(f32x4*)(kt + 8 * l15 + 4 * (g & 1) + 128 * (g >> 1)) = acc[0];
    {
      const int t0 = g < 2 ? 256 + 4 * l15 + 64 * g : 384 + 2 * l15 + 64 * (g - 2);
      const int t1 = g < 2 ? t0 + 2 : t0 + 32;
      *(f32x2*)(kt + t0) = (f32x2){acc[1][0], acc[1][1]};
      *(f32x2*)(kt + t1) = (f32x2){acc[1][2], acc[1][3]};
    }
#pragma unroll
    for (int i = 0; i < 4; ++i) kt[512 + l15 + 16 * (4 * g + i)] = acc[2][i];
  }
  STAMP(2);
  u32x4 ua; u32x2 un;
  bool uok = false;
  const u32 tw = (tag & 0xffffu) << 16;
  __builtin_amdgcn_s_setprio(BPRIO2);
  bar_l();
  __builtin_amdgcn_sched_barrier(0);
  STAMP(3);
  if (tid < 384) {
    const int ch = tid >= 192 ? 1 : 0, t4 = (tid - 192 * ch) * 4;
    const float* k0 = (const float*)(lds + 4 * ch * KG_SLAB) + t4;
    f32x4 s = *(const f32x4*)k0;
#pragma unroll
    for (int q = 1; q < 4; ++q) s += *(const f32x4*)(k0 + q * (KG_SLAB / 4));
    *(u32x2*)(lds + B_KTAB + ch * 1536 + t4 * 2) = (u32x2){pk2(s[0], s[1]), pk2(s[2], s[3])};
  } else if (w == 7) {
#pragma unroll 1
    for (int it = 0; it < MAXSPIN; ++it) {
      const u32 xfl = ld4c(rWS, (lane & 31) * 128 + (ch0 >> 6) * 4096, WS_XF);
      if (__all(xfl == tag)) break;
      __builtin_amdgcn_s_sleep(2);
    }
  }
  STAMP(10);
  bar_l();
  if (!uok) { ua = ld16c(rWS, uvo, WS_XT); un = ld8c(rWS, uvo + 16, WS_XT); }
  __builtin_amdgcn_sched_barrier(0);
  {
    const int mp = cq ^ 1, cl = w & 3;
    const char* kt = lds + B_KTAB + cch * 1536;
    u32 o[4];
    if (cl >= 2) {
      const u32 lo = *(const u16*)(kt + (mp + 384) * 2), hi = *(const u16*)(kt + (mp + 385) * 2);
      const u32 tie = (mp & 1) ? hi : lo;
      o[0] = hi | (hi << 16); o[1] = hi | (tie << 16); o[2] = o[3] = lo | (lo << 16);
    } else if (cl == 1) {
      const u32 ab = *(const u32*)(kt + (2 * mp + 256) * 2), c = *(const u16*)(kt + (2 * mp + 258) * 2);
      const u32 a = ab & 0xffffu, b = ab >> 16;
      o[0] = c | (c << 16); o[1] = b | (b << 16); o[2] = b | (a << 16); o[3] = a | (a << 16);
    } else {
      const u32x4 v = *(const u32x4*)(kt + 16 * mp);
      const u32x2 k = *(const u32x2*)(kt + 256 + 8 * mp);
      const u32 k4 = *(const u16*)(kt + 256 + 8 * mp + 8);
      const bool id = mp < 32;
      o[0] = id ? __builtin_amdgcn_alignbit(v[3], v[3], 16) : (k4 | (k[1] & 0xffff0000u));
      o[1] = id ? __builtin_amdgcn_alignbit(v[2], v[2], 16) : ((k[1] & 0xffffu) | (k[1] << 16));
      o[2] = id ? __builtin_amdgcn_alignbit(v[1], v[1], 16) : ((k[1] & 0xffffu) | (k[0] & 0xffff0000u));
      o[3] = id ? __builtin_amdgcn_alignbit(v[0], v[0], 16) : ((k[0] & 0xffffu) | (k[0] << 16));
    }
    char* kr = lds + B_KR + cch * 4608;
    *(u32x4*)(kr + 512 + cq * 16) = (u32x4){o[0], o[1], o[2], o[3]};
    if (cq < 32) *(u32x4*)(kr + cq * 16) = (u32x4){0, 0, 0, 0};
  }
  __builtin_amdgcn_sched_barrier(0);
  STAMP(12);
  {
    const u32 e[6] = {ua[0], ua[1], ua[2], ua[3], cq < 255 ? un[0] : 0u, cq < 255 ? un[1] : 0u};
    char* base = lds + B_UC + cch * UCH + (16 + cq * 8) * 2;
    u32 sh[5];
#pragma unroll
    for (int q = 0; q < 5; ++q) sh[q] = __builtin_amdgcn_alignbit(e[q + 1], e[q], 16);
    *(u32x2*)(base + UC0) = (u32x2){e[0], e[1]}; *(u32x2*)(base + UC0 + 8) = (u32x2){e[2], e[3]};
    *(u32x4*)(base + UC1) = (u32x4){sh[0], sh[1], sh[2], sh[3]};
    { u32 c1 = e[1], c2 = e[2]; asm("" : "+v"(c1), "+v"(c2));
      *(u32x4*)(base + UC2) = (u32x4){c1, c2, e[3], e[4]}; }
    *(u32x4*)(base + UC3) = (u32x4){sh[1], sh[2], sh[3], sh[4]};
    if (cq == 0) {
      char* b0 = lds + B_UC + cch * UCH;
      const u32x4 z = {0, 0, 0, 0};
      *(u32x2*)(b0 + UC0) = (u32x2){0, 0}; *(u32x2*)(b0 + UC0 + 8) = (u32x2){0, 0}; *(u32x2*)(b0 + UC0 + 16) = (u32x2){0, 0}; *(u32x2*)(b0 + UC0 + 24) = (u32x2){0, 0};
      *(u32x4*)(b0 + UC1) = z; *(u32x4*)(b0 + UC1 + 16) = (u32x4){0, 0, 0, e[0] << 16};
      *(u32x4*)(b0 + UC2) = z; *(u32x4*)(b0 + UC2 + 16) = (u32x4){0, 0, 0, e[0]};
      *(u32x4*)(b0 + UC3) = z; *(u32x4*)(b0 + UC3 + 16) = (u32x4){0, 0, e[0] << 16, sh[0]};
    }
  }
  if (w == 5 || w == 6) atouch4(mkrs4(p.in[3], D * D * 2), ldsaddr(lds) + B_DUM, lane * 128, (jb * 2 + (w - 5)) * 7168);
  bar_l();
  STAMP(4);
  __builtin_amdgcn_s_setprio(BPRIO3);
  {
    const int ch = w >> 2, rh = (w >> 1) & 1, sh = w & 1;
    const int idx0 = 8 * g + l15 + 1, cc = idx0 & 3;
    const u32 ucb = cc == 0 ? UC0 : (cc == 1 ? UC1 : (cc == 2 ? UC2 : UC3));
    const LAS char* abase = (const LAS char*)(lds + B_UC + ch * UCH + ucb + (idx0 & ~3) * 2 + rh * 256);
    const char* bbase = lds + B_KR + ch * 4608 + (256 + 16 * (l15 - (g >> 1)) + 8 * (g & 1)) * 2 - rh * 256;
    f32x4 c4[4];
#pragma unroll
    for (int i = 0; i < 4; ++i) c4[i] = (f32x4){0, 0, 0, 0};
    if (sh == 0) {
#pragma unroll 1
      for (int rr = 0; rr < 4; ++rr) {
        u32x4 af[8], bf[8];
#pragma unroll
        for (int a = 0; a < 8; ++a) {
          const LAS char* pa = abase + (256 * a) * 2 + rr * 64;
          u32x2 lo = *(const volatile LAS u32x2*)pa, hi = *(const volatile LAS u32x2*)(pa + 8);
          af[a] = (u32x4){lo[0], lo[1], hi[0], hi[1]};
          bf[a] = *(const u32x4*)(bbase + (256 * a) * 2 - rr * 64);
        }
        c4[0] = mfma16(af[0], bf[0], c4[0]);
        c4[1] = mfma16(af[0], bf[1], c4[1]); c4[1] = mfma16(af[1], bf[0], c4[1]);
#pragma unroll
        for (int a = 0; a < 8; ++a) {
          if (a < 7) c4[2] = mfma16(af[a], bf[6 - a], c4[2]);
          c4[3] = mfma16(af[a], bf[7 - a], c4[3]);
        }
      }
    } else {
#pragma unroll 1
      for (int rr = 0; rr < 4; ++rr) {
        u32x4 af[6], bf[6];
#pragma unroll
        for (int a = 0; a < 6; ++a) {
          const LAS char* pa = abase + (256 * a) * 2 + rr * 64;
          u32x2 lo = *(const volatile LAS u32x2*)pa, hi = *(const volatile LAS u32x2*)(pa + 8);
          af[a] = (u32x4){lo[0], lo[1], hi[0], hi[1]};
          bf[a] = *(const u32x4*)(bbase + (256 * a) * 2 - rr * 64);
        }
#pragma unroll
        for (int a = 0; a < 6; ++a) {
          if (a < 3) c4[0] = mfma16(af[a], bf[2 - a], c4[0]);
          if (a < 4) c4[1] = mfma16(af[a], bf[3 - a], c4[1]);
          if (a < 5) c4[2] = mfma16(af[a], bf[4 - a], c4[2]);
          c4[3] = mfma16(af[a], bf[5 - a], c4[3]);
        }
      }
    }
    STAMP(5);
    __builtin_amdgcn_s_setprio(3);
    const f32x4 e0 = c4[0], e1 = c4[1], e2 = c4[2], e3 = c4[3];
    f32x4* xw = (f32x4*)(lds + B_RED + w * 2048) + lane;
    const int pt = sh == 0 ? (rh ? 6 : 0) : (rh ? 4 : 2);
    const int tb = 256 * (pt + (g & 1)) + 16 * l15 + 8 * (g >> 1);
    const char* ub0 = lds + B_UC + ch * UCH + UC0 + (16 + tb) * 2;
    const u32x2 x0 = *(const u32x2*)ub0, x1 = *(const u32x2*)(ub0 + 8);
    xw[0] = rh ? e0 : e2; xw[64] = rh ? e1 : e3;
    const float dsk = bflo(dskip);
    const u32 xx[4] = {x0[0], x0[1], x1[0], x1[1]};
    u32 xd[4];
#pragma unroll
    for (int q = 0; q < 4; ++q) xd[q] = pk2(smul(bflo(xx[q]), dsk), smul(bfhi(xx[q]), dsk));
    bar_l();
    const f32x4* xr = (const f32x4*)(lds + B_RED + (w ^ 2) * 2048) + lane;
    f32x4 v0 = xr[0], v1 = xr[64];
    __builtin_amdgcn_s_setprio(3);
    v0 += rh ? e2 : e0; v1 += rh ? e3 : e1;
    auto s0 = __builtin_amdgcn_permlane16_swap(pk2(v0[0], v0[1]), pk2(v1[0], v1[1]), false, false);
    auto s1 = __builtin_amdgcn_permlane16_swap(pk2(v0[2], v0[3]), pk2(v1[2], v1[3]), false, false);
    const u32 yy[4] = {s0[0], s1[0], s0[1], s1[1]};
    u32x4 ob;
#pragma unroll
    for (int q = 0; q < 4; ++q) ob[q] = pk2(bflo(yy[q]) + bflo(xd[q]), bfhi(yy[q]) + bfhi(xd[q]));
    st16c(ob, rWS, (ch0 + ch) * YSTR + tb * 2, WS_YT);
    STAMP(11);
    waitv<0>();
    BAR();
    if (w == 0) st16c((u32x4){tw, tw, tw, tw}, rWS, (ch0 >> 1) * 1024 + lane * 16, WS_YF);
    STAMP(6);
  }
  DBGCNT(st_[8] = tent;)
  STAMP_FLUSH(ws);
  asm volatile(".Lcode_end:");
}

__global__ __launch_bounds__(512, 4) void fused(Params p) {
  const u32 tent = 0;
  unsigned long long pc0; u32 clen;
  asm volatile("s_getpc_b64 %0\n.Lpc_here:\n\ts_mov_b32 %1, .Lcode_end-.Lpc_here" : "=s"(pc0), "=s"(clen));
  __shared__ __attribute__((aligned(16))) char lds[LDS_BYTES];
  const int b = blockIdx.x, x8 = b & 7, jj = b >> 3;
  __builtin_amdgcn_s_setprio(3);
  if (jj == 0) atouch4(mkrs4((const void*)pc0, clen & ~3u), ldsaddr(lds) + B_DUM, threadIdx.x << 6, 0);
  __builtin_amdgcn_sched_barrier(0);
  BPre q;
  int jb = jj; asm volatile("" : "+s"(jb));
  if (jb < 28) preB(p, x8, jb, lds, q);
  if (__builtin_expect(jj >= 28, 1)) roleA(p, x8, jj & 31, lds, tent);
  else roleB(p, x8, jj, lds, tent, q);
}

extern "C" void kernel_launch(void* const* d_in, const int* in_sizes, int n_in, void* d_out, int out_size, void* d_ws, size_t ws_size, hipStream_t stream) {
  Params p;
  for (int i = 0; i < 14; ++i) p.in[i] = (const u16*)d_in[i];
  p.ws = (char*)d_ws; p.out = (u16*)d_out;
  fused<<<480, 512, 0, stream>>>(p);
}
```
